```python
import torch

def _softmax(x, axis=-1):
    return torch.softmax(x, dim=axis)
def _gelu(x):
    return torch.nn.functional.gelu(x, approximate="tanh")
def _silu(x):
    return torch.nn.functional.silu(x)
def _sigmoid(x):
    return torch.sigmoid(x)
def _layernorm(x, axis=-1, eps=1e-5):
    m = x.mean(dim=axis, keepdim=True)
    v = x.var(dim=axis, keepdim=True, unbiased=False)
    return (x - m) / torch.sqrt(v + eps)
def _scatter_add(tgt, idx, src, axis=0):
    return tgt.clone().index_add_(axis, idx, src)
def _segment_sum(src, seg, n):
    out = torch.zeros((n,) + src.shape[1:], dtype=src.dtype)
    return out.index_add_(0, seg, src)
def _pad(x, pw):
    flat = tuple(v for pair in reversed(pw) for v in pair)
    return torch.nn.functional.pad(x, flat)

kernel_name = 'parallel_sum__mha_block__dense_activation_squared_relu__prompt'

def setup_inputs():

    _w = (lambda a: (lambda t: t.to(torch.bfloat16) if t.is_floating_point() else t)(torch.as_tensor(a)))
    def _A_setup():
        B, S, D, H = 1, 2048, 448, 16
        return {
            "x": _w((lambda *s: torch.randn(*s, dtype=torch.bfloat16) * 0.05)(B, S, D)),
            "wqkv": _w((lambda *s: torch.randn(*s, dtype=torch.bfloat16) * 0.2)(D, 3 * D)),
            "wo": _w((lambda *s: torch.randn(*s, dtype=torch.bfloat16) * 0.1)(D, D)),
        }
    _A_in = _A_setup()

    def _B_setup():
        B, S, D, F = 1, 2048, 448, 1792
        return {
            "x": _w((lambda *s: torch.randn(*s, dtype=torch.bfloat16) * 0.05)(B, S, D)),
            "w1": _w((lambda *s: torch.randn(*s, dtype=torch.bfloat16) * 0.1)(D, F)),
            "b1": _w((lambda *s: torch.randn(*s, dtype=torch.bfloat16) * 0.8)(F)),
            "w2": _w((lambda *s: torch.randn(*s, dtype=torch.bfloat16) * 0.1)(F, D)),
            "b2": _w((lambda *s: torch.randn(*s, dtype=torch.bfloat16) * 12.8)(D)),
        }
    _B_in = _B_setup()

    return {
        "A_x": _A_in["x"],
        "A_wqkv": _A_in["wqkv"],
        "A_wo": _A_in["wo"],
        "B_x": _B_in["x"],
        "B_w1": _B_in["w1"],
        "B_b1": _B_in["b1"],
        "B_w2": _B_in["w2"],
        "B_b2": _B_in["b2"],
    }

def reference(A_x, A_wqkv, A_wo, B_x, B_w1, B_b1, B_w2, B_b2):
    def _A_ref(x, wqkv, wo):
        B, S, D = x.shape
        H = 16
        Dh = D // H
        qkv = (lambda x, *d: x.permute(*d))((x @ wqkv).reshape(B, S, 3, H, Dh), 2, 0, 3, 1, 4)
        q, k, v = qkv[0], qkv[1], qkv[2]
        scores = torch.einsum("bhqd,bhkd->bhqk", q, k) * (Dh ** -0.5)
        p = _softmax(scores, axis=-1)
        o = torch.einsum("bhqk,bhkd->bhqd", p, v)
        return (lambda x, *d: x.permute(*d))(o, 0, 2, 1, 3).reshape(B, S, D) @ wo

    def _B_ref(x, w1, b1, w2, b2):
        h = torch.einsum("bsd,df->bsf", x, w1) + b1
        zero = torch.zeros_like(h)
        r = torch.where(h > zero, h, zero)
        h = r * r
        out = torch.einsum("bsf,fd->bsd", h, w2) + b2
        return out

    x = A_x
    xn = _layernorm(x, axis=-1)
    return x + _A_ref(xn, A_wqkv, A_wo) + _B_ref(xn, B_w1, B_b1, B_w2, B_b2)
```

```cpp
#include <hip/hip_runtime.h>
#include <hip/hip_bf16.h>
typedef unsigned short u16;
__device__ __forceinline__ float bf2f(u16 b){ return __uint_as_float(((unsigned)b)<<16); }
__device__ __forceinline__ u16 f2bf(float f){ unsigned u=__float_as_uint(f); u += 0x7FFF + ((u>>16)&1); return (u16)(u>>16); }
constexpr int S=2048, D=448, H=16, DH=28, F=1792;
__global__ void k_ln(const u16* x, u16* xn){
  int row=blockIdx.x; int lane=threadIdx.x; float v[7]; float s=0;
  for(int j=0;j<7;j++){ v[j]=bf2f(x[row*D+lane+64*j]); s+=v[j]; }
  for(int o=1;o<64;o<<=1) s+=__shfl_xor(s,o);
  float m=s/D; float q=0; for(int j=0;j<7;j++){ v[j]-=m; q+=v[j]*v[j]; }
  for(int o=1;o<64;o<<=1) q+=__shfl_xor(q,o);
  float r=rsqrtf(q/D+1e-5f);
  for(int j=0;j<7;j++) xn[row*D+lane+64*j]=f2bf(v[j]*r);
}
__global__ void k_gemm(const u16* A, const u16* B, const u16* bias, u16* C, int N, int K, int mode){
  int row=blockIdx.y; int col=blockIdx.x*64+threadIdx.x; float acc=0;
  for(int k=0;k<K;k++) acc+=bf2f(A[row*K+k])*bf2f(B[k*N+col]);
  if(mode==1){ acc+=bf2f(bias[col]); acc=acc>0?acc:0; acc=acc*acc; }
  C[row*N+col]=f2bf(acc);
}
__global__ void k_attn(const u16* qkv, u16* o){
  int q=blockIdx.x, h=blockIdx.y, lane=threadIdx.x;
  __shared__ float p[S];
  float qv[DH]; for(int d=0;d<DH;d++) qv[d]=bf2f(qkv[q*1344+h*DH+d]);
  float m=-1e30f;
  for(int k=lane;k<S;k+=64){ float s=0; for(int d=0;d<DH;d++) s+=qv[d]*bf2f(qkv[k*1344+448+h*DH+d]); s*=0.18898223650461362f; p[k]=s; m=fmaxf(m,s); }
  for(int of=1;of<64;of<<=1) m=fmaxf(m,__shfl_xor(m,of));
  float l=0; for(int k=lane;k<S;k+=64){ float e=__expf(p[k]-m); p[k]=e; l+=e; }
  for(int of=1;of<64;of<<=1) l+=__shfl_xor(l,of);
  __syncthreads();
  if(lane<DH){ float a=0; for(int k=0;k<S;k++) a+=p[k]*bf2f(qkv[k*1344+896+h*DH+lane]); o[q*D+h*DH+lane]=f2bf(a/l); }
}
__global__ void k_final(const u16* x,const u16* o,const u16* wo,const u16* h,const u16* w2,const u16* b2,u16* out){
  int row=blockIdx.y; int col=blockIdx.x*64+threadIdx.x; float a=0,b=0;
  for(int k=0;k<D;k++) a+=bf2f(o[row*D+k])*bf2f(wo[k*D+col]);
  for(int k=0;k<F;k++) b+=bf2f(h[row*F+k])*bf2f(w2[k*D+col]);
  out[row*D+col]=f2bf(bf2f(x[row*D+col])+a+b+bf2f(b2[col]));
}
extern "C" void kernel_launch(void* const* d_in, const int* in_sizes, int n_in, void* d_out, int out_size, void* d_ws, size_t ws_size, hipStream_t stream) {
  const u16* x=(const u16*)d_in[0]; const u16* wqkv=(const u16*)d_in[1]; const u16* wo=(const u16*)d_in[2];
  const u16* w1=(const u16*)d_in[4]; const u16* b1=(const u16*)d_in[5]; const u16* w2=(const u16*)d_in[6]; const u16* b2=(const u16*)d_in[7];
  u16* ws=(u16*)d_ws; u16* xn=ws; u16* qkv=xn+S*D; u16* hb=qkv+S*1344; u16* o=hb+S*F;
  k_ln<<<S,64,0,stream>>>(x,xn);
  k_gemm<<<dim3(1344/64,S),64,0,stream>>>(xn,wqkv,nullptr,qkv,1344,D,0);
  k_gemm<<<dim3(F/64,S),64,0,stream>>>(xn,w1,b1,hb,F,D,1);
  k_attn<<<dim3(S,H),64,0,stream>>>(qkv,o);
  k_final<<<dim3(D/64,S),64,0,stream>>>(x,o,wo,hb,w2,b2,(u16*)d_out);
}
```

```cpp
#include <hip/hip_runtime.h>
#include <stdint.h>

struct KArgs { const void* x; const void* wqkv; const void* wo; const void* w1; const void* b1; const void* w2; const void* b2; void* out; void* ws; };

__global__ __launch_bounds__(256, 1) void fused_block_kernel(KArgs args) {
    __shared__ __attribute__((aligned(1024))) char lds[163840];
    unsigned tid = threadIdx.x;
    asm volatile(
        "k_begin_%=:\n"
        "s_load_dwordx16 s[60:75], %1, 0\n"
        "s_load_dwordx2 s[76:77], %1, 64\n"
        "v_lshrrev_b32 v2, 6, v0\n"
        "v_and_b32 v3, 63, v0\n"
        "v_readfirstlane_b32 s78, v2\n"
        "v_lshlrev_b32 v1, 4, v3\n"
        "s_and_b32 s94, %2, 7\n"
        "s_lshr_b32 s95, %2, 3\n"
        "s_getpc_b64 s[8:9]\n"
        "k_pc_%=:\n"
        "s_sub_u32 s8, s8, k_pc_%=-k_begin_%=\n"
        "s_subb_u32 s9, s9, 0\n"
        "s_and_b32 s9, s9, 0xffff\n"
        "s_mov_b32 s10, k_end_%=-k_begin_%=\n"
        "s_mov_b32 s11, 0x20000\n"
        "s_lshl_b32 s96, s95, 2\n"
        "s_add_u32 s96, s96, s78\n"
        "s_lshl_b32 s96, s96, 10\n"
        "buffer_load_dwordx4 a[112:115], v1, s[8:11], s96 offen\n"
        "s_and_b32 s96, s94, 1\n"
        "s_lshl_b32 s96, s96, 3\n"
        "s_and_b32 s97, s95, 7\n"
        "s_add_u32 s79, s96, s97\n"
        "s_lshr_b32 s96, s94, 1\n"
        "s_lshl_b32 s96, s96, 2\n"
        "s_lshr_b32 s97, s95, 3\n"
        "s_add_u32 s80, s96, s97\n"
        "s_lshl_b32 s96, s94, 2\n"
        "s_and_b32 s97, s95, 3\n"
        "s_add_u32 s85, s96, s97\n"
        "s_lshr_b32 s86, s95, 2\n"
        "s_lshl_b32 s96, 1, s80\n"
        "s_sub_u32 s96, s96, 1\n"
        "s_and_b32 s97, s96, 0x2800\n"
        "s_bcnt1_i32_b32 s97, s97\n"
        "s_sub_u32 s81, s80, s97\n"
        "s_and_b32 s97, s96, 0x254\n"
        "s_bcnt1_i32_b32 s97, s97\n"
        "s_lshl_b32 s82, s80, 1\n"
        "s_sub_u32 s82, s82, s97\n"
        "s_mul_i32 s83, s78, 40960\n"
        "s_add_u32 s83, s83, %3\n"
        "s_lshl_b32 s84, s79, 2\n"
        "s_add_u32 s84, s84, s78\n"
        "s_mul_i32 s94, s84, 28672\n"
        "s_mul_i32 s95, s78, 43008\n"
        "s_lshl_b32 s97, s81, 7\n"
        "s_add_u32 s95, s95, s97\n"
        "s_add_u32 s95, s95, 896\n"
        "s_mov_b32 s96, 0x2800\n"
        "s_mov_b32 s97, 0x254\n"
        "v_lshrrev_b32 v4, 3, v3\n"
        "v_and_b32 v5, 7, v3\n"
        "v_lshrrev_b32 v6, 4, v3\n"
        "v_xor_b32 v6, v5, v6\n"
        "v_mul_u32_u24 v7, 896, v4\n"
        "v_lshl_add_u32 v14, v6, 4, v7\n"
        "v_xor_b32 v8, 64, v14\n"
        "v_add_u32 v15, 6144, v8\n"
        "v_add_u32 v16, 12288, v14\n"
        "v_add_u32 v17, 18432, v8\n"
        "v_bfe_u32 v6, v3, 4, 1\n"
        "v_lshlrev_b32 v6, 2, v6\n"
        "v_xor_b32 v6, v5, v6\n"
        "v_lshlrev_b32 v9, 4, v6\n"
        "v_mov_b32 v7, 2688\n"
        "v_mad_u32_u24 v18, v4, v7, v9\n"
        "v_add_u32 v19, 20480, v18\n"
        "s_mov_b32 s42, 0x7000\n"
        "s_mov_b32 s43, 0x20000\n"
        "s_bitcmp0_b32 s96, s80\n"
        "s_cselect_b32 s14, 0x106800, 0\n"
        "s_mov_b32 s15, 0x20000\n"
        "s_add_u32 s98, s79, s80\n"
        "s_mul_i32 s99, s98, 37\n"
        "s_lshr_b32 s99, s99, 8\n"
        "s_mul_i32 s99, s99, 7\n"
        "s_sub_u32 s98, s98, s99\n"
        "s_mul_i32 s98, s98, 4\n"
        "s_mov_b32 s4, 0x06543210\n"
        "s_mov_b32 s5, 0x654321\n"
        "s_lshr_b64 s[4:5], s[4:5], s98\n"
        "s_waitcnt lgkmcnt(0)\n"
        "s_add_u32 s40, s60, s94\n"
        "s_addc_u32 s41, s61, 0\n"
        "s_add_u32 s12, s62, s95\n"
        "s_addc_u32 s13, s63, 0\n"
        "s_mov_b32 s8, s60\n"
        "s_mov_b32 s9, s61\n"
        "s_mov_b32 s16, s62\n"
        "s_mov_b32 s17, s63\n"
        "s_mov_b32 s63, s4\n"
        "s_bfe_u32 s61, s63, 0x40000\n"
        "s_add_u32 m0, s83, 0x0\n"
        "s_lshl_b32 s60, s61, 7\n"
        "buffer_load_dwordx4 v14, s[40:43], s60 offen lds\n"
        "buffer_load_dwordx4 v15, s[40:43], s60 offen offset:1024 lds\n"
        "buffer_load_dwordx4 v16, s[40:43], s60 offen offset:2048 lds\n"
        "buffer_load_dwordx4 v17, s[40:43], s60 offen offset:3072 lds\n"
        "s_add_u32 m0, s83, 0x6000\n"
        "s_mul_i32 s60, s61, 0x2a000\n"
        "buffer_load_dwordx4 v18, s[12:15], s60 offen lds\n"
        "buffer_load_dwordx4 v19, s[12:15], s60 offen offset:1024 lds\n"
        "s_add_u32 s76, s76, 0xfffff\n"
        "s_addc_u32 s77, s77, 0\n"
        "s_and_b32 s76, s76, 0xfff00000\n"
        "s_mov_b32 s28, s76\n"
        "s_and_b32 s29, s77, 0xffff\n"
        "s_mov_b32 s30, 0xe40000\n"
        "s_mov_b32 s31, 0x20000\n"
        "buffer_load_dwordx2 v[24:25], off, s[28:31], 0 sc1\n"
        "s_mov_b32 s10, 0x1c0000\n"
        "s_mov_b32 s11, 0x20000\n"
        "s_mov_b32 s18, 0x106800\n"
        "s_mov_b32 s19, 0x20000\n"
        "s_mul_i32 s98, s80, 56\n"
        "s_and_b32 s98, s98, -16\n"
        "s_mul_i32 s99, s78, 43008\n"
        "s_add_u32 s98, s98, s99\n"
        "s_add_u32 s16, s16, s98\n"
        "s_addc_u32 s17, s17, 0\n"
        "s_bfe_u32 s61, s63, 0x40004\n"
        "s_add_u32 m0, s83, 0x1000\n"
        "s_lshl_b32 s60, s61, 7\n"
        "buffer_load_dwordx4 v14, s[40:43], s60 offen lds\n"
        "s_mul_i32 s99, s78, 57344\n"
        "s_lshl_b32 s98, s82, 7\n"
        "s_add_u32 s99, s99, s98\n"
        "s_add_u32 s20, s66, s99\n"
        "s_addc_u32 s21, s67, 0\n"
        "s_mov_b32 s22, 0x15e000\n"
        "s_mov_b32 s23, 0x20000\n"
        "s_add_u32 s24, s20, 128\n"
        "s_addc_u32 s25, s21, 0\n"
        "s_bitcmp0_b32 s97, s80\n"
        "s_cselect_b32 s26, 0x15e000, 0\n"
        "s_mov_b32 s27, 0x20000\n"
        "buffer_load_dwordx4 v15, s[40:43], s60 offen offset:1024 lds\n"
        "s_mov_b32 s48, s64\n"
        "s_and_b32 s49, s65, 0xffff\n"
        "s_mov_b32 s50, 0x62000\n"
        "s_mov_b32 s51, 0x20000\n"
        "s_mov_b32 s36, s76\n"
        "s_and_b32 s37, s77, 0xffff\n"
        "s_bitcmp0_b32 s97, s80\n"
        "s_cselect_b32 s38, 0xe40000, 0\n"
        "s_mov_b32 s39, 0x20000\n"
        "s_mov_b32 s32, s76\n"
        "buffer_load_dwordx4 v16, s[40:43], s60 offen offset:2048 lds\n"
        "s_and_b32 s33, s77, 0xffff\n"
        "s_bitcmp0_b32 s96, s80\n"
        "s_cselect_b32 s34, 0xe40000, 0\n"
        "s_mov_b32 s35, 0x20000\n"
        "s_mov_b32 s44, s70\n"
        "s_and_b32 s45, s71, 0xffff\n"
        "s_cmp_lt_u32 s86, 7\n"
        "s_cselect_b32 s46, 0x188000, 0\n"
        "s_mov_b32 s47, 0x20000\n"
        "s_mov_b32 s52, s68\n"
        "s_and_b32 s53, s69, 0xffff\n"
        "buffer_load_dwordx4 v17, s[40:43], s60 offen offset:3072 lds\n"
        "s_mov_b32 s54, 0xe00\n"
        "s_mov_b32 s55, 0x20000\n"
        "s_mov_b32 s56, s74\n"
        "s_and_b32 s57, s75, 0xffff\n"
        "s_mov_b32 s58, 0x1c0000\n"
        "s_mov_b32 s59, 0x20000\n"
        "v_and_b32 v237, 31, v3\n"
        "v_lshrrev_b32 v238, 5, v3\n"
        "v_lshlrev_b32 v7, 1, v237\n"
        "s_add_u32 m0, s83, 0x6800\n"
        "s_mul_i32 s60, s61, 0x2a000\n"
        "buffer_load_dwordx4 v18, s[12:15], s60 offen lds\n"
        "v_lshl_add_u32 v7, v238, 16, v7\n"
        "buffer_load_ushort a108, v7, s[52:55], s98 offen offset:0\n"
        "buffer_load_ushort a109, v7, s[52:55], s98 offen offset:64\n"
        "buffer_load_ushort a110, v7, s[52:55], s98 offen offset:128\n"
        "buffer_load_ushort a111, v7, s[52:55], s98 offen offset:192\n"
        "s_mov_b32 s52, s72\n"
        "s_and_b32 s53, s73, 0xffff\n"
        "s_mov_b32 s54, 896\n"
        "s_mul_i32 s66, s84, 65536\n"
        "buffer_load_dwordx4 v19, s[12:15], s60 offen offset:1024 lds\n"
        "s_add_u32 s66, s66, 0x100000\n"
        "s_mov_b32 s90, 0xffff0000\n"
        "s_mov_b32 s91, 0x3f803f80\n"
        "s_mov_b32 s92, 0\n"
        "s_mov_b32 s93, -1\n"
        "v_mov_b32 v7, 3584\n"
        "v_mad_u32_u24 v20, v4, v7, v9\n"
        "v_add_u32 v21, 27648, v20\n"
        "v_lshrrev_b32 v7, 2, v3\n"
        "s_bfe_u32 s61, s63, 0x40008\n"
        "s_add_u32 m0, s83, 0x2000\n"
        "s_lshl_b32 s60, s61, 7\n"
        "buffer_load_dwordx4 v14, s[40:43], s60 offen lds\n"
        "v_mul_u32_u24 v10, 2688, v7\n"
        "v_and_b32 v8, 3, v3\n"
        "v_lshl_add_u32 v22, v8, 4, v10\n"
        "v_cmp_ne_u32 vcc, 0, v3\n"
        "v_mov_b32 v10, 0x40000000\n"
        "v_xor_b32 v7, v7, v3\n"
        "v_cndmask_b32 v23, 0, v10, vcc\n"
        "v_bfe_u32 v7, v7, 2, 1\n"
        "v_cmp_eq_u32 vcc, 0, v7\n"
        "buffer_load_dwordx4 v15, s[40:43], s60 offen offset:1024 lds\n"
        "v_mov_b32 v7, s91\n"
        "v_mov_b32 v28, 0\n"
        "v_cndmask_b32 v7, 0, v7, vcc\n"
        "v_mov_b32 v29, 0\n"
        "v_accvgpr_write_b32 a96, v7\n"
        "v_accvgpr_write_b32 a97, v7\n"
        "v_accvgpr_write_b32 a98, v7\n"
        "v_accvgpr_write_b32 a99, v7\n"
        "v_mov_b32 v114, 0\n"
        "buffer_load_dwordx4 v16, s[40:43], s60 offen offset:2048 lds\n"
        "v_mov_b32 v115, 0\n"
        "v_mov_b32 v117, 0\n"
        "v_mov_b32 v118, 0\n"
        "v_mov_b32 v119, 0\n"
        "v_bfe_u32 v10, v3, 2, 2\n"
        "v_lshlrev_b32 v11, 7, v10\n"
        "v_lshl_add_u32 v11, v238, 10, v11\n"
        "v_lshrrev_b32 v10, 1, v10\n"
        "v_lshl_add_u32 v11, v10, 6, v11\n"
        "buffer_load_dwordx4 v17, s[40:43], s60 offen offset:3072 lds\n"
        "v_bfe_u32 v10, v3, 4, 1\n"
        "v_lshl_add_u32 v11, v10, 5, v11\n"
        "v_lshl_add_u32 v30, v8, 3, v11\n"
        "v_xor_b32 v31, 64, v30\n"
        "v_bfe_u32 v232, v3, 1, 3\n"
        "v_lshlrev_b32 v233, 7, v237\n"
        "v_add_u32 v233, s83, v233\n"
        "s_add_u32 s96, s78, 0\n"
        "s_and_b32 s96, s96, 3\n"
        "s_add_u32 m0, s83, 0x7000\n"
        "s_mul_i32 s60, s61, 0x2a000\n"
        "buffer_load_dwordx4 v18, s[12:15], s60 offen lds\n"
        "s_lshl_b32 s97, s96, 1\n"
        "s_mul_i32 s96, s96, 40960\n"
        "s_add_u32 s96, s96, %3\n"
        "v_add_u32 v234, s97, v238\n"
        "v_add_u32 v6, s96, v30\n"
        "v_xor_b32 v234, v234, v232\n"
        "v_add_u32 v10, s96, v31\n"
        "v_lshl_add_u32 v2, v234, 4, v233\n"
        "s_add_u32 s96, s78, 1\n"
        "buffer_load_dwordx4 v19, s[12:15], s60 offen offset:1024 lds\n"
        "s_and_b32 s96, s96, 3\n"
        "s_lshl_b32 s97, s96, 1\n"
        "s_mul_i32 s96, s96, 40960\n"
        "s_add_u32 s96, s96, %3\n"
        "v_add_u32 v234, s97, v238\n"
        "v_add_u32 v7, s96, v30\n"
        "v_xor_b32 v234, v234, v232\n"
        "v_add_u32 v11, s96, v31\n"
        "v_lshl_add_u32 v3, v234, 4, v233\n"
        "s_bfe_u32 s61, s63, 0x4000c\n"
        "s_add_u32 m0, s83, 0x3000\n"
        "s_lshl_b32 s60, s61, 7\n"
        "buffer_load_dwordx4 v14, s[40:43], s60 offen lds\n"
        "s_add_u32 s96, s78, 2\n"
        "s_and_b32 s96, s96, 3\n"
        "s_lshl_b32 s97, s96, 1\n"
        "s_mul_i32 s96, s96, 40960\n"
        "s_add_u32 s96, s96, %3\n"
        "v_add_u32 v234, s97, v238\n"
        "v_add_u32 v8, s96, v30\n"
        "v_xor_b32 v234, v234, v232\n"
        "v_add_u32 v12, s96, v31\n"
        "buffer_load_dwordx4 v15, s[40:43], s60 offen offset:1024 lds\n"
        "v_lshl_add_u32 v4, v234, 4, v233\n"
        "s_add_u32 s96, s78, 3\n"
        "s_and_b32 s96, s96, 3\n"
        "s_lshl_b32 s97, s96, 1\n"
        "s_mul_i32 s96, s96, 40960\n"
        "s_add_u32 s96, s96, %3\n"
        "v_add_u32 v234, s97, v238\n"
        "v_add_u32 v9, s96, v30\n"
        "v_xor_b32 v234, v234, v232\n"
        "buffer_load_dwordx4 v16, s[40:43], s60 offen offset:2048 lds\n"
        "v_add_u32 v13, s96, v31\n"
        "v_lshl_add_u32 v5, v234, 4, v233\n"
        "buffer_load_dwordx4 v17, s[40:43], s60 offen offset:3072 lds\n"
        "s_add_u32 m0, s83, 0x7800\n"
        "s_mul_i32 s60, s61, 0x2a000\n"
        "buffer_load_dwordx4 v18, s[12:15], s60 offen lds\n"
        "buffer_load_dwordx4 v19, s[12:15], s60 offen offset:1024 lds\n"
        "s_cmp_lt_u32 s81, 7\n"
        "s_cbranch_scc0 role_v_%=\n"
        "s_lshl_b32 s96, s81, 12\n"
        "s_add_u32 s66, s66, s96\n"
        "s_waitcnt vmcnt(22)\n"
        "s_barrier\n"
        "ds_read_b128 v[128:131], v2 offset:0\n"
        "ds_read_b128 v[132:135], v3 offset:0\n"
        "ds_read_b128 v[136:139], v4 offset:0\n"
        "ds_read_b128 v[140:143], v5 offset:0\n"
        "ds_read_b64_tr_b16 a[0:1], v6 offset:24576\n"
        "ds_read_b64_tr_b16 a[2:3], v6 offset:25088\n"
        "ds_read_b64_tr_b16 a[4:5], v10 offset:24576\n"
        "ds_read_b64_tr_b16 a[6:7], v10 offset:25088\n"
        "ds_read_b64_tr_b16 a[8:9], v7 offset:24576\n"
        "ds_read_b64_tr_b16 a[10:11], v7 offset:25088\n"
        "ds_read_b64_tr_b16 a[12:13], v11 offset:24576\n"
        "ds_read_b64_tr_b16 a[14:15], v11 offset:25088\n"
        "ds_read_b64_tr_b16 a[16:17], v8 offset:24576\n"
        "ds_read_b64_tr_b16 a[18:19], v8 offset:25088\n"
        "ds_read_b64_tr_b16 a[20:21], v12 offset:24576\n"
        "ds_read_b64_tr_b16 a[22:23], v12 offset:25088\n"
        "ds_read_b64_tr_b16 a[24:25], v9 offset:24576\n"
        "ds_read_b64_tr_b16 a[26:27], v9 offset:25088\n"
        "ds_read_b64_tr_b16 a[28:29], v13 offset:24576\n"
        "ds_read_b64_tr_b16 a[30:31], v13 offset:25088\n"
        "v_readfirstlane_b32 s88, v24\n"
        "v_readfirstlane_b32 s89, v25\n"
        "s_add_u32 s88, s88, 1\n"
        "s_addc_u32 s89, s89, 0\n"
        "v_mov_b32 v24, s88\n"
        "v_mov_b32 v25, s89\n"
        "s_waitcnt vmcnt(12) lgkmcnt(0)\n"
        "s_barrier\n"
        "v_mfma_f32_32x32x16_bf16 v[48:63], a[0:3], v[128:131], 0\n"
        "s_bfe_u32 s61, s63, 0x40010\n"
        "s_add_u32 m0, s83, 0x4000\n"
        "s_lshl_b32 s60, s61, 7\n"
        "buffer_load_dwordx4 v14, s[40:43], s60 offen lds\n"
        "ds_read_b128 v[144:147], v2 offset:4096\n"
        "v_mfma_f32_32x32x16_bf16 v[64:79], a[4:7], v[128:131], 0\n"
        "ds_read_b128 v[148:151], v3 offset:4096\n"
        "ds_read_b128 v[152:155], v4 offset:4096\n"
        "ds_read_b128 v[156:159], v5 offset:4096\n"
        "ds_read_b64_tr_b16 a[32:33], v6 offset:26624\n"
        "v_mfma_f32_16x16x32_bf16 a[100:103], a[96:99], a[0:3], 0\n"
        "ds_read_b64_tr_b16 a[34:35], v6 offset:27136\n"
        "buffer_load_dwordx4 v15, s[40:43], s60 offen offset:1024 lds\n"
        "v_mfma_f32_16x16x32_bf16 a[104:107], a[96:99], a[4:7], 0\n"
        "ds_read_b64_tr_b16 a[36:37], v10 offset:26624\n"
        "ds_read_b64_tr_b16 a[38:39], v10 offset:27136\n"
        "v_mfma_f32_32x32x16_bf16 v[48:63], a[8:11], v[132:135], v[48:63]\n"
        "ds_read_b64_tr_b16 a[40:41], v7 offset:26624\n"
        "ds_read_b64_tr_b16 a[42:43], v7 offset:27136\n"
        "ds_read_b64_tr_b16 a[44:45], v11 offset:26624\n"
        "ds_read_b64_tr_b16 a[46:47], v11 offset:27136\n"
        "v_mfma_f32_32x32x16_bf16 v[64:79], a[12:15], v[132:135], v[64:79]\n"
        "buffer_load_dwordx4 v16, s[40:43], s60 offen offset:2048 lds\n"
        "ds_read_b64_tr_b16 a[48:49], v8 offset:26624\n"
        "ds_read_b64_tr_b16 a[50:51], v8 offset:27136\n"
        "ds_read_b64_tr_b16 a[52:53], v12 offset:26624\n"
        "v_mfma_f32_32x32x16_bf16 v[48:63], a[16:19], v[136:139], v[48:63]\n"
        "ds_read_b64_tr_b16 a[54:55], v12 offset:27136\n"
        "ds_read_b64_tr_b16 a[56:57], v9 offset:26624\n"
        "ds_read_b64_tr_b16 a[58:59], v9 offset:27136\n"
        "ds_read_b64_tr_b16 a[60:61], v13 offset:26624\n"
        "v_mfma_f32_32x32x16_bf16 v[64:79], a[20:23], v[136:139], v[64:79]\n"
        "buffer_load_dwordx4 v17, s[40:43], s60 offen offset:3072 lds\n"
        "s_add_u32 m0, s83, 0x8000\n"
        "s_mul_i32 s60, s61, 0x2a000\n"
        "ds_read_b64_tr_b16 a[62:63], v13 offset:27136\n"
        "v_mfma_f32_32x32x16_bf16 v[48:63], a[24:27], v[140:143], v[48:63]\n"
        "v_mfma_f32_32x32x16_bf16 v[64:79], a[28:31], v[140:143], v[64:79]\n"
        "buffer_load_dwordx4 v18, s[12:15], s60 offen lds\n"
        "v_dot2c_f32_bf16 v28, s91, v128\n"
        "v_dot2c_f32_bf16 v29, v128, v128\n"
        "v_dot2c_f32_bf16 v28, s91, v129\n"
        "v_dot2c_f32_bf16 v29, v129, v129\n"
        "v_dot2c_f32_bf16 v28, s91, v130\n"
        "v_dot2c_f32_bf16 v29, v130, v130\n"
        "v_dot2c_f32_bf16 v28, s91, v131\n"
        "v_dot2c_f32_bf16 v29, v131, v131\n"
        "v_dot2c_f32_bf16 v28, s91, v132\n"
        "v_dot2c_f32_bf16 v29, v132, v132\n"
        "v_dot2c_f32_bf16 v28, s91, v133\n"
        "v_dot2c_f32_bf16 v29, v133, v133\n"
        "v_dot2c_f32_bf16 v28, s91, v134\n"
        "v_dot2c_f32_bf16 v29, v134, v134\n"
        "v_dot2c_f32_bf16 v28, s91, v135\n"
        "v_dot2c_f32_bf16 v29, v135, v135\n"
        "buffer_load_dwordx4 v19, s[12:15], s60 offen offset:1024 lds\n"
        "s_bfe_u32 s61, s63, 0x40014\n"
        "s_add_u32 m0, s83, 0x5000\n"
        "v_dot2c_f32_bf16 v28, s91, v136\n"
        "v_dot2c_f32_bf16 v29, v136, v136\n"
        "v_dot2c_f32_bf16 v28, s91, v137\n"
        "v_dot2c_f32_bf16 v29, v137, v137\n"
        "v_dot2c_f32_bf16 v28, s91, v138\n"
        "v_dot2c_f32_bf16 v29, v138, v138\n"
        "v_dot2c_f32_bf16 v28, s91, v139\n"
        "v_dot2c_f32_bf16 v29, v139, v139\n"
        "s_lshl_b32 s60, s61, 7\n"
        "v_dot2c_f32_bf16 v28, s91, v140\n"
        "v_dot2c_f32_bf16 v29, v140, v140\n"
        "v_dot2c_f32_bf16 v28, s91, v141\n"
        "v_dot2c_f32_bf16 v29, v141, v141\n"
        "v_dot2c_f32_bf16 v28, s91, v142\n"
        "v_dot2c_f32_bf16 v29, v142, v142\n"
        "v_dot2c_f32_bf16 v28, s91, v143\n"
        "v_dot2c_f32_bf16 v29, v143, v143\n"
        "buffer_load_dwordx4 v14, s[40:43], s60 offen lds\n"
        "s_waitcnt vmcnt(13) lgkmcnt(0)\n"
        "s_barrier\n"
        "v_mfma_f32_32x32x16_bf16 v[48:63], a[32:35], v[144:147], v[48:63]\n"
        "ds_read_b128 v[160:163], v2 offset:8192\n"
        "ds_read_b128 v[164:167], v3 offset:8192\n"
        "ds_read_b128 v[168:171], v4 offset:8192\n"
        "ds_read_b128 v[172:175], v5 offset:8192\n"
        "v_mfma_f32_32x32x16_bf16 v[64:79], a[36:39], v[144:147], v[64:79]\n"
        "buffer_load_dwordx4 v15, s[40:43], s60 offen offset:1024 lds\n"
        "ds_read_b64_tr_b16 a[0:1], v6 offset:28672\n"
        "ds_read_b64_tr_b16 a[2:3], v6 offset:29184\n"
        "ds_read_b64_tr_b16 a[4:5], v10 offset:28672\n"
        "v_mfma_f32_16x16x32_bf16 a[100:103], a[96:99], a[32:35], a[100:103]\n"
        "ds_read_b64_tr_b16 a[6:7], v10 offset:29184\n"
        "ds_read_b64_tr_b16 a[8:9], v7 offset:28672\n"
        "v_mfma_f32_16x16x32_bf16 a[104:107], a[96:99], a[36:39], a[104:107]\n"
        "ds_read_b64_tr_b16 a[10:11], v7 offset:29184\n"
        "buffer_load_dwordx4 v16, s[40:43], s60 offen offset:2048 lds\n"
        "v_mfma_f32_32x32x16_bf16 v[48:63], a[40:43], v[148:151], v[48:63]\n"
        "ds_read_b64_tr_b16 a[12:13], v11 offset:28672\n"
        "ds_read_b64_tr_b16 a[14:15], v11 offset:29184\n"
        "ds_read_b64_tr_b16 a[16:17], v8 offset:28672\n"
        "ds_read_b64_tr_b16 a[18:19], v8 offset:29184\n"
        "v_mfma_f32_32x32x16_bf16 v[64:79], a[44:47], v[148:151], v[64:79]\n"
        "ds_read_b64_tr_b16 a[20:21], v12 offset:28672\n"
        "ds_read_b64_tr_b16 a[22:23], v12 offset:29184\n"
        "buffer_load_dwordx4 v17, s[40:43], s60 offen offset:3072 lds\n"
        "s_add_u32 m0, s83, 0x8800\n"
        "s_mul_i32 s60, s61, 0x2a000\n"
        "v_mfma_f32_32x32x16_bf16 v[48:63], a[48:51], v[152:155], v[48:63]\n"
        "ds_read_b64_tr_b16 a[24:25], v9 offset:28672\n"
        "ds_read_b64_tr_b16 a[26:27], v9 offset:29184\n"
        "ds_read_b64_tr_b16 a[28:29], v13 offset:28672\n"
        "ds_read_b64_tr_b16 a[30:31], v13 offset:29184\n"
        "v_mfma_f32_32x32x16_bf16 v[64:79], a[52:55], v[152:155], v[64:79]\n"
        "v_mfma_f32_32x32x16_bf16 v[48:63], a[56:59], v[156:159], v[48:63]\n"
        "buffer_load_dwordx4 v18, s[12:15], s60 offen lds\n"
        "v_mfma_f32_32x32x16_bf16 v[64:79], a[60:63], v[156:159], v[64:79]\n"
        "v_dot2c_f32_bf16 v28, s91, v144\n"
        "v_dot2c_f32_bf16 v29, v144, v144\n"
        "v_dot2c_f32_bf16 v28, s91, v145\n"
        "v_dot2c_f32_bf16 v29, v145, v145\n"
        "v_dot2c_f32_bf16 v28, s91, v146\n"
        "v_dot2c_f32_bf16 v29, v146, v146\n"
        "v_dot2c_f32_bf16 v28, s91, v147\n"
        "v_dot2c_f32_bf16 v29, v147, v147\n"
        "buffer_load_dwordx4 v19, s[12:15], s60 offen offset:1024 lds\n"
        "s_bfe_u32 s61, s63, 0x40018\n"
        "s_add_u32 m0, s83, 0x0\n"
        "v_dot2c_f32_bf16 v28, s91, v148\n"
        "v_dot2c_f32_bf16 v29, v148, v148\n"
        "v_dot2c_f32_bf16 v28, s91, v149\n"
        "v_dot2c_f32_bf16 v29, v149, v149\n"
        "v_dot2c_f32_bf16 v28, s91, v150\n"
        "v_dot2c_f32_bf16 v29, v150, v150\n"
        "v_dot2c_f32_bf16 v28, s91, v151\n"
        "v_dot2c_f32_bf16 v29, v151, v151\n"
        "s_lshl_b32 s60, s61, 7\n"
        "v_dot2c_f32_bf16 v28, s91, v152\n"
        "v_dot2c_f32_bf16 v29, v152, v152\n"
        "v_dot2c_f32_bf16 v28, s91, v153\n"
        "v_dot2c_f32_bf16 v29, v153, v153\n"
        "v_dot2c_f32_bf16 v28, s91, v154\n"
        "v_dot2c_f32_bf16 v29, v154, v154\n"
        "v_dot2c_f32_bf16 v28, s91, v155\n"
        "v_dot2c_f32_bf16 v29, v155, v155\n"
        "buffer_load_dwordx4 v14, s[40:43], s60 offen lds\n"
        "v_dot2c_f32_bf16 v28, s91, v156\n"
        "v_dot2c_f32_bf16 v29, v156, v156\n"
        "v_dot2c_f32_bf16 v28, s91, v157\n"
        "v_dot2c_f32_bf16 v29, v157, v157\n"
        "v_dot2c_f32_bf16 v28, s91, v158\n"
        "v_dot2c_f32_bf16 v29, v158, v158\n"
        "v_dot2c_f32_bf16 v28, s91, v159\n"
        "v_dot2c_f32_bf16 v29, v159, v159\n"
        "s_waitcnt vmcnt(13) lgkmcnt(0)\n"
        "s_barrier\n"
        "v_mfma_f32_32x32x16_bf16 v[48:63], a[0:3], v[160:163], v[48:63]\n"
        "ds_read_b128 v[176:179], v2 offset:12288\n"
        "buffer_load_dwordx4 v15, s[40:43], s60 offen offset:1024 lds\n"
        "ds_read_b128 v[180:183], v3 offset:12288\n"
        "ds_read_b128 v[184:187], v4 offset:12288\n"
        "v_mfma_f32_32x32x16_bf16 v[64:79], a[4:7], v[160:163], v[64:79]\n"
        "ds_read_b128 v[188:191], v5 offset:12288\n"
        "ds_read_b64_tr_b16 a[32:33], v6 offset:30720\n"
        "ds_read_b64_tr_b16 a[34:35], v6 offset:31232\n"
        "ds_read_b64_tr_b16 a[36:37], v10 offset:30720\n"
        "v_mfma_f32_16x16x32_bf16 a[100:103], a[96:99], a[0:3], a[100:103]\n"
        "buffer_load_dwordx4 v16, s[40:43], s60 offen offset:2048 lds\n"
        "ds_read_b64_tr_b16 a[38:39], v10 offset:31232\n"
        "v_mfma_f32_16x16x32_bf16 a[104:107], a[96:99], a[4:7], a[104:107]\n"
        "ds_read_b64_tr_b16 a[40:41], v7 offset:30720\n"
        "ds_read_b64_tr_b16 a[42:43], v7 offset:31232\n"
        "v_mfma_f32_32x32x16_bf16 v[48:63], a[8:11], v[164:167], v[48:63]\n"
        "ds_read_b64_tr_b16 a[44:45], v11 offset:30720\n"
        "ds_read_b64_tr_b16 a[46:47], v11 offset:31232\n"
        "ds_read_b64_tr_b16 a[48:49], v8 offset:30720\n"
        "buffer_load_dwordx4 v17, s[40:43], s60 offen offset:3072 lds\n"
        "v_mfma_f32_32x32x16_bf16 v[64:79], a[12:15], v[164:167], v[64:79]\n"
        "s_add_u32 m0, s83, 0x9000\n"
        "s_mul_i32 s60, s61, 0x2a000\n"
        "ds_read_b64_tr_b16 a[50:51], v8 offset:31232\n"
        "ds_read_b64_tr_b16 a[52:53], v12 offset:30720\n"
        "ds_read_b64_tr_b16 a[54:55], v12 offset:31232\n"
        "v_mfma_f32_32x32x16_bf16 v[48:63], a[16:19], v[168:171], v[48:63]\n"
        "ds_read_b64_tr_b16 a[56:57], v9 offset:30720\n"
        "ds_read_b64_tr_b16 a[58:59], v9 offset:31232\n"
        "buffer_load_dwordx4 v18, s[12:15], s60 offen lds\n"
        "ds_read_b64_tr_b16 a[60:61], v13 offset:30720\n"
        "v_mfma_f32_32x32x16_bf16 v[64:79], a[20:23], v[168:171], v[64:79]\n"
        "ds_read_b64_tr_b16 a[62:63], v13 offset:31232\n"
        "v_mfma_f32_32x32x16_bf16 v[48:63], a[24:27], v[172:175], v[48:63]\n"
        "v_mfma_f32_32x32x16_bf16 v[64:79], a[28:31], v[172:175], v[64:79]\n"
        "buffer_load_dwordx4 v19, s[12:15], s60 offen offset:1024 lds\n"
        "s_bfe_u32 s61, s63, 0x40000\n"
        "s_add_u32 m0, s83, 0x1000\n"
        "s_mul_i32 s60, s61, 0x38000\n"
        "v_dot2c_f32_bf16 v28, s91, v160\n"
        "v_dot2c_f32_bf16 v29, v160, v160\n"
        "v_dot2c_f32_bf16 v28, s91, v161\n"
        "v_dot2c_f32_bf16 v29, v161, v161\n"
        "v_dot2c_f32_bf16 v28, s91, v162\n"
        "v_dot2c_f32_bf16 v29, v162, v162\n"
        "v_dot2c_f32_bf16 v28, s91, v163\n"
        "v_dot2c_f32_bf16 v29, v163, v163\n"
        "v_dot2c_f32_bf16 v28, s91, v164\n"
        "v_dot2c_f32_bf16 v29, v164, v164\n"
        "v_dot2c_f32_bf16 v28, s91, v165\n"
        "v_dot2c_f32_bf16 v29, v165, v165\n"
        "v_dot2c_f32_bf16 v28, s91, v166\n"
        "v_dot2c_f32_bf16 v29, v166, v166\n"
        "v_dot2c_f32_bf16 v28, s91, v167\n"
        "v_dot2c_f32_bf16 v29, v167, v167\n"
        "buffer_load_dwordx4 v20, s[20:23], s60 offen lds\n"
        "v_dot2c_f32_bf16 v28, s91, v168\n"
        "v_dot2c_f32_bf16 v29, v168, v168\n"
        "v_dot2c_f32_bf16 v28, s91, v169\n"
        "v_dot2c_f32_bf16 v29, v169, v169\n"
        "v_dot2c_f32_bf16 v28, s91, v170\n"
        "v_dot2c_f32_bf16 v29, v170, v170\n"
        "v_dot2c_f32_bf16 v28, s91, v171\n"
        "v_dot2c_f32_bf16 v29, v171, v171\n"
        "v_dot2c_f32_bf16 v28, s91, v172\n"
        "v_dot2c_f32_bf16 v29, v172, v172\n"
        "v_dot2c_f32_bf16 v28, s91, v173\n"
        "v_dot2c_f32_bf16 v29, v173, v173\n"
        "v_dot2c_f32_bf16 v28, s91, v174\n"
        "v_dot2c_f32_bf16 v29, v174, v174\n"
        "v_dot2c_f32_bf16 v28, s91, v175\n"
        "v_dot2c_f32_bf16 v29, v175, v175\n"
        "buffer_load_dwordx4 v21, s[20:23], s60 offen offset:1024 lds\n"
        "s_bfe_u32 s61, s63, 0x40004\n"
        "s_add_u32 m0, s83, 0x1800\n"
        "s_waitcnt vmcnt(14) lgkmcnt(0)\n"
        "s_barrier\n"
        "v_mfma_f32_32x32x16_bf16 v[48:63], a[32:35], v[176:179], v[48:63]\n"
        "s_mul_i32 s60, s61, 0x38000\n"
        "ds_read_b128 v[192:195], v2 offset:16384\n"
        "ds_read_b128 v[196:199], v3 offset:16384\n"
        "ds_read_b128 v[200:203], v4 offset:16384\n"
        "v_mfma_f32_32x32x16_bf16 v[64:79], a[36:39], v[176:179], v[64:79]\n"
        "buffer_load_dwordx4 v20, s[20:23], s60 offen lds\n"
        "ds_read_b128 v[204:207], v5 offset:16384\n"
        "ds_read_b64_tr_b16 a[0:1], v6 offset:32768\n"
        "ds_read_b64_tr_b16 a[2:3], v6 offset:33280\n"
        "v_mfma_f32_16x16x32_bf16 a[100:103], a[96:99], a[32:35], a[100:103]\n"
        "ds_read_b64_tr_b16 a[4:5], v10 offset:32768\n"
        "ds_read_b64_tr_b16 a[6:7], v10 offset:33280\n"
        "v_mfma_f32_16x16x32_bf16 a[104:107], a[96:99], a[36:39], a[104:107]\n"
        "ds_read_b64_tr_b16 a[8:9], v7 offset:32768\n"
        "buffer_load_dwordx4 v21, s[20:23], s60 offen offset:1024 lds\n"
        "v_mfma_f32_32x32x16_bf16 v[48:63], a[40:43], v[180:183], v[48:63]\n"
        "s_bfe_u32 s61, s63, 0x40008\n"
        "s_add_u32 m0, s83, 0x2000\n"
        "s_mul_i32 s60, s61, 0x38000\n"
        "ds_read_b64_tr_b16 a[10:11], v7 offset:33280\n"
        "ds_read_b64_tr_b16 a[12:13], v11 offset:32768\n"
        "v_mfma_f32_32x32x16_bf16 v[64:79], a[44:47], v[180:183], v[64:79]\n"
        "ds_read_b64_tr_b16 a[14:15], v11 offset:33280\n"
        "ds_read_b64_tr_b16 a[16:17], v8 offset:32768\n"
        "ds_read_b64_tr_b16 a[18:19], v8 offset:33280\n"
        "buffer_load_dwordx4 v20, s[20:23], s60 offen lds\n"
        "v_mfma_f32_32x32x16_bf16 v[48:63], a[48:51], v[184:187], v[48:63]\n"
        "ds_read_b64_tr_b16 a[20:21], v12 offset:32768\n"
        "ds_read_b64_tr_b16 a[22:23], v12 offset:33280\n"
        "ds_read_b64_tr_b16 a[24:25], v9 offset:32768\n"
        "ds_read_b64_tr_b16 a[26:27], v9 offset:33280\n"
        "v_mfma_f32_32x32x16_bf16 v[64:79], a[52:55], v[184:187], v[64:79]\n"
        "ds_read_b64_tr_b16 a[28:29], v13 offset:32768\n"
        "ds_read_b64_tr_b16 a[30:31], v13 offset:33280\n"
        "buffer_load_dwordx4 v21, s[20:23], s60 offen offset:1024 lds\n"
        "s_bfe_u32 s61, s63, 0x4000c\n"
        "s_add_u32 m0, s83, 0x2800\n"
        "v_mfma_f32_32x32x16_bf16 v[48:63], a[56:59], v[188:191], v[48:63]\n"
        "s_mul_i32 s60, s61, 0x38000\n"
        "v_mfma_f32_32x32x16_bf16 v[64:79], a[60:63], v[188:191], v[64:79]\n"
        "v_dot2c_f32_bf16 v28, s91, v176\n"
        "v_dot2c_f32_bf16 v29, v176, v176\n"
        "v_dot2c_f32_bf16 v28, s91, v177\n"
        "v_dot2c_f32_bf16 v29, v177, v177\n"
        "v_dot2c_f32_bf16 v28, s91, v178\n"
        "v_dot2c_f32_bf16 v29, v178, v178\n"
        "v_dot2c_f32_bf16 v28, s91, v179\n"
        "v_dot2c_f32_bf16 v29, v179, v179\n"
        "buffer_load_dwordx4 v20, s[20:23], s60 offen lds\n"
        "v_dot2c_f32_bf16 v28, s91, v180\n"
        "v_dot2c_f32_bf16 v29, v180, v180\n"
        "v_dot2c_f32_bf16 v28, s91, v181\n"
        "v_dot2c_f32_bf16 v29, v181, v181\n"
        "v_dot2c_f32_bf16 v28, s91, v182\n"
        "v_dot2c_f32_bf16 v29, v182, v182\n"
        "v_dot2c_f32_bf16 v28, s91, v183\n"
        "v_dot2c_f32_bf16 v29, v183, v183\n"
        "v_dot2c_f32_bf16 v28, s91, v184\n"
        "v_dot2c_f32_bf16 v29, v184, v184\n"
        "v_dot2c_f32_bf16 v28, s91, v185\n"
        "v_dot2c_f32_bf16 v29, v185, v185\n"
        "v_dot2c_f32_bf16 v28, s91, v186\n"
        "v_dot2c_f32_bf16 v29, v186, v186\n"
        "v_dot2c_f32_bf16 v28, s91, v187\n"
        "v_dot2c_f32_bf16 v29, v187, v187\n"
        "buffer_load_dwordx4 v21, s[20:23], s60 offen offset:1024 lds\n"
        "s_bfe_u32 s61, s63, 0x40010\n"
        "s_add_u32 m0, s83, 0x3000\n"
        "v_dot2c_f32_bf16 v28, s91, v188\n"
        "v_dot2c_f32_bf16 v29, v188, v188\n"
        "v_dot2c_f32_bf16 v28, s91, v189\n"
        "v_dot2c_f32_bf16 v29, v189, v189\n"
        "v_dot2c_f32_bf16 v28, s91, v190\n"
        "v_dot2c_f32_bf16 v29, v190, v190\n"
        "v_dot2c_f32_bf16 v28, s91, v191\n"
        "v_dot2c_f32_bf16 v29, v191, v191\n"
        "s_mul_i32 s60, s61, 0x38000\n"
        "s_waitcnt vmcnt(14) lgkmcnt(0)\n"
        "s_barrier\n"
        "v_mfma_f32_32x32x16_bf16 v[48:63], a[0:3], v[192:195], v[48:63]\n"
        "buffer_load_dwordx4 v20, s[20:23], s60 offen lds\n"
        "ds_read_b128 v[208:211], v2 offset:20480\n"
        "ds_read_b128 v[212:215], v3 offset:20480\n"
        "ds_read_b128 v[216:219], v4 offset:20480\n"
        "v_mfma_f32_32x32x16_bf16 v[64:79], a[4:7], v[192:195], v[64:79]\n"
        "ds_read_b128 v[220:223], v5 offset:20480\n"
        "ds_read_b64_tr_b16 a[32:33], v6 offset:34816\n"
        "ds_read_b64_tr_b16 a[34:35], v6 offset:35328\n"
        "ds_read_b64_tr_b16 a[36:37], v10 offset:34816\n"
        "v_mfma_f32_16x16x32_bf16 a[100:103], a[96:99], a[0:3], a[100:103]\n"
        "buffer_load_dwordx4 v21, s[20:23], s60 offen offset:1024 lds\n"
        "s_bfe_u32 s61, s63, 0x40014\n"
        "s_add_u32 m0, s83, 0x3800\n"
        "v_mfma_f32_16x16x32_bf16 a[104:107], a[96:99], a[4:7], a[104:107]\n"
        "s_mul_i32 s60, s61, 0x38000\n"
        "ds_read_b64_tr_b16 a[38:39], v10 offset:35328\n"
        "v_mfma_f32_32x32x16_bf16 v[48:63], a[8:11], v[196:199], v[48:63]\n"
        "ds_read_b64_tr_b16 a[40:41], v7 offset:34816\n"
        "ds_read_b64_tr_b16 a[42:43], v7 offset:35328\n"
        "ds_read_b64_tr_b16 a[44:45], v11 offset:34816\n"
        "ds_read_b64_tr_b16 a[46:47], v11 offset:35328\n"
        "v_mfma_f32_32x32x16_bf16 v[64:79], a[12:15], v[196:199], v[64:79]\n"
        "buffer_load_dwordx4 v20, s[20:23], s60 offen lds\n"
        "ds_read_b64_tr_b16 a[48:49], v8 offset:34816\n"
        "ds_read_b64_tr_b16 a[50:51], v8 offset:35328\n"
        "ds_read_b64_tr_b16 a[52:53], v12 offset:34816\n"
        "v_mfma_f32_32x32x16_bf16 v[48:63], a[16:19], v[200:203], v[48:63]\n"
        "ds_read_b64_tr_b16 a[54:55], v12 offset:35328\n"
        "ds_read_b64_tr_b16 a[56:57], v9 offset:34816\n"
        "ds_read_b64_tr_b16 a[58:59], v9 offset:35328\n"
        "ds_read_b64_tr_b16 a[60:61], v13 offset:34816\n"
        "v_mfma_f32_32x32x16_bf16 v[64:79], a[20:23], v[200:203], v[64:79]\n"
        "buffer_load_dwordx4 v21, s[20:23], s60 offen offset:1024 lds\n"
        "s_bfe_u32 s61, s63, 0x40018\n"
        "s_add_u32 m0, s83, 0x4000\n"
        "s_mul_i32 s60, s61, 0x38000\n"
        "ds_read_b64_tr_b16 a[62:63], v13 offset:35328\n"
        "v_mfma_f32_32x32x16_bf16 v[48:63], a[24:27], v[204:207], v[48:63]\n"
        "v_add_u32 v19, %3, v1\n"
        "v_add_u32 v19, 38912, v19\n"
        "s_lshl_b32 s96, s78, 2\n"
        "v_add_u32 v18, s96, v19\n"
        "v_mfma_f32_32x32x16_bf16 v[64:79], a[28:31], v[204:207], v[64:79]\n"
        "buffer_load_dwordx4 v20, s[20:23], s60 offen lds\n"
        "v_dot2c_f32_bf16 v28, s91, v192\n"
        "v_dot2c_f32_bf16 v29, v192, v192\n"
        "v_dot2c_f32_bf16 v28, s91, v193\n"
        "v_dot2c_f32_bf16 v29, v193, v193\n"
        "v_dot2c_f32_bf16 v28, s91, v194\n"
        "v_dot2c_f32_bf16 v29, v194, v194\n"
        "v_dot2c_f32_bf16 v28, s91, v195\n"
        "v_dot2c_f32_bf16 v29, v195, v195\n"
        "v_dot2c_f32_bf16 v28, s91, v196\n"
        "v_dot2c_f32_bf16 v29, v196, v196\n"
        "v_dot2c_f32_bf16 v28, s91, v197\n"
        "v_dot2c_f32_bf16 v29, v197, v197\n"
        "v_dot2c_f32_bf16 v28, s91, v198\n"
        "v_dot2c_f32_bf16 v29, v198, v198\n"
        "v_dot2c_f32_bf16 v28, s91, v199\n"
        "v_dot2c_f32_bf16 v29, v199, v199\n"
        "buffer_load_dwordx4 v21, s[20:23], s60 offen offset:1024 lds\n"
        "s_bfe_u32 s61, s63, 0x40000\n"
        "s_add_u32 m0, s83, 0x4800\n"
        "v_dot2c_f32_bf16 v28, s91, v200\n"
        "v_dot2c_f32_bf16 v29, v200, v200\n"
        "v_dot2c_f32_bf16 v28, s91, v201\n"
        "v_dot2c_f32_bf16 v29, v201, v201\n"
        "v_dot2c_f32_bf16 v28, s91, v202\n"
        "v_dot2c_f32_bf16 v29, v202, v202\n"
        "v_dot2c_f32_bf16 v28, s91, v203\n"
        "v_dot2c_f32_bf16 v29, v203, v203\n"
        "s_mul_i32 s60, s61, 0x2a000\n"
        "v_dot2c_f32_bf16 v28, s91, v204\n"
        "v_dot2c_f32_bf16 v29, v204, v204\n"
        "v_dot2c_f32_bf16 v28, s91, v205\n"
        "v_dot2c_f32_bf16 v29, v205, v205\n"
        "v_dot2c_f32_bf16 v28, s91, v206\n"
        "v_dot2c_f32_bf16 v29, v206, v206\n"
        "v_dot2c_f32_bf16 v28, s91, v207\n"
        "v_dot2c_f32_bf16 v29, v207, v207\n"
        "s_waitcnt vmcnt(14) lgkmcnt(0)\n"
        "s_barrier\n"
        "v_mfma_f32_32x32x16_bf16 v[48:63], a[32:35], v[208:211], v[48:63]\n"
        "ds_read_b128 v[224:227], v2 offset:0\n"
        "ds_read_b128 v[228:231], v3 offset:0\n"
        "ds_read_b128 v[232:235], v4 offset:0\n"
        "ds_read_b128 v[236:239], v5 offset:0\n"
        "v_mfma_f32_32x32x16_bf16 v[64:79], a[36:39], v[208:211], v[64:79]\n"
        "buffer_load_dwordx4 v22, s[16:19], s60 offen lds\n"
        "s_bfe_u32 s61, s63, 0x40004\n"
        "s_add_u32 m0, s83, 0x4c00\n"
        "s_mul_i32 s60, s61, 0x2a000\n"
        "ds_read_b64_tr_b16 a[0:1], v6 offset:36864\n"
        "v_mfma_f32_16x16x32_bf16 a[100:103], a[96:99], a[32:35], a[100:103]\n"
        "ds_read_b64_tr_b16 a[2:3], v6 offset:37376\n"
        "ds_read_b64_tr_b16 a[4:5], v10 offset:36864\n"
        "v_mfma_f32_16x16x32_bf16 a[104:107], a[96:99], a[36:39], a[104:107]\n"
        "ds_read_b64_tr_b16 a[6:7], v10 offset:37376\n"
        "ds_read_b64_tr_b16 a[8:9], v7 offset:36864\n"
        "v_mfma_f32_32x32x16_bf16 v[48:63], a[40:43], v[212:215], v[48:63]\n"
        "ds_read_b64_tr_b16 a[10:11], v7 offset:37376\n"
        "ds_read_b64_tr_b16 a[12:13], v11 offset:36864\n"
        "ds_read_b64_tr_b16 a[14:15], v11 offset:37376\n"
        "ds_read_b64_tr_b16 a[16:17], v8 offset:36864\n"
        "v_mfma_f32_32x32x16_bf16 v[64:79], a[44:47], v[212:215], v[64:79]\n"
        "ds_read_b64_tr_b16 a[18:19], v8 offset:37376\n"
        "ds_read_b64_tr_b16 a[20:21], v12 offset:36864\n"
        "ds_read_b64_tr_b16 a[22:23], v12 offset:37376\n"
        "ds_read_b64_tr_b16 a[24:25], v9 offset:36864\n"
        "v_mfma_f32_32x32x16_bf16 v[48:63], a[48:51], v[216:219], v[48:63]\n"
        "buffer_load_dwordx4 v22, s[16:19], s60 offen lds\n"
        "s_bfe_u32 s61, s63, 0x40008\n"
        "s_add_u32 m0, s83, 0x5000\n"
        "s_mul_i32 s60, s61, 0x2a000\n"
        "ds_read_b64_tr_b16 a[26:27], v9 offset:37376\n"
        "v_mfma_f32_32x32x16_bf16 v[64:79], a[52:55], v[216:219], v[64:79]\n"
        "ds_read_b64_tr_b16 a[28:29], v13 offset:36864\n"
        "ds_read_b64_tr_b16 a[30:31], v13 offset:37376\n"
        "v_mfma_f32_32x32x16_bf16 v[48:63], a[56:59], v[220:223], v[48:63]\n"
        "v_mfma_f32_32x32x16_bf16 v[64:79], a[60:63], v[220:223], v[64:79]\n"
        "v_dot2c_f32_bf16 v28, s91, v208\n"
        "v_dot2c_f32_bf16 v29, v208, v208\n"
        "v_dot2c_f32_bf16 v28, s91, v209\n"
        "v_dot2c_f32_bf16 v29, v209, v209\n"
        "v_dot2c_f32_bf16 v28, s91, v210\n"
        "v_dot2c_f32_bf16 v29, v210, v210\n"
        "v_dot2c_f32_bf16 v28, s91, v211\n"
        "v_dot2c_f32_bf16 v29, v211, v211\n"
        "v_dot2c_f32_bf16 v28, s91, v212\n"
        "v_dot2c_f32_bf16 v29, v212, v212\n"
        "v_dot2c_f32_bf16 v28, s91, v213\n"
        "v_dot2c_f32_bf16 v29, v213, v213\n"
        "v_dot2c_f32_bf16 v28, s91, v214\n"
        "v_dot2c_f32_bf16 v29, v214, v214\n"
        "v_dot2c_f32_bf16 v28, s91, v215\n"
        "v_dot2c_f32_bf16 v29, v215, v215\n"
        "buffer_load_dwordx4 v22, s[16:19], s60 offen lds\n"
        "s_bfe_u32 s61, s63, 0x4000c\n"
        "s_add_u32 m0, s83, 0x5400\n"
        "v_dot2c_f32_bf16 v28, s91, v216\n"
        "v_dot2c_f32_bf16 v29, v216, v216\n"
        "v_dot2c_f32_bf16 v28, s91, v217\n"
        "v_dot2c_f32_bf16 v29, v217, v217\n"
        "v_dot2c_f32_bf16 v28, s91, v218\n"
        "v_dot2c_f32_bf16 v29, v218, v218\n"
        "v_dot2c_f32_bf16 v28, s91, v219\n"
        "v_dot2c_f32_bf16 v29, v219, v219\n"
        "s_mul_i32 s60, s61, 0x2a000\n"
        "v_dot2c_f32_bf16 v28, s91, v220\n"
        "v_dot2c_f32_bf16 v29, v220, v220\n"
        "v_dot2c_f32_bf16 v28, s91, v221\n"
        "v_dot2c_f32_bf16 v29, v221, v221\n"
        "v_dot2c_f32_bf16 v28, s91, v222\n"
        "v_dot2c_f32_bf16 v29, v222, v222\n"
        "v_dot2c_f32_bf16 v28, s91, v223\n"
        "v_dot2c_f32_bf16 v29, v223, v223\n"
        "s_waitcnt lgkmcnt(0)\n"
        "v_dot2c_f32_bf16 v28, s91, v224\n"
        "v_dot2c_f32_bf16 v29, v224, v224\n"
        "v_dot2c_f32_bf16 v28, s91, v225\n"
        "v_dot2c_f32_bf16 v29, v225, v225\n"
        "v_dot2c_f32_bf16 v28, s91, v226\n"
        "v_dot2c_f32_bf16 v29, v226, v226\n"
        "v_dot2c_f32_bf16 v28, s91, v227\n"
        "v_dot2c_f32_bf16 v29, v227, v227\n"
        "v_dot2c_f32_bf16 v28, s91, v228\n"
        "v_dot2c_f32_bf16 v29, v228, v228\n"
        "v_dot2c_f32_bf16 v28, s91, v229\n"
        "v_dot2c_f32_bf16 v29, v229, v229\n"
        "v_dot2c_f32_bf16 v28, s91, v230\n"
        "v_dot2c_f32_bf16 v29, v230, v230\n"
        "v_dot2c_f32_bf16 v28, s91, v231\n"
        "v_dot2c_f32_bf16 v29, v231, v231\n"
        "v_dot2c_f32_bf16 v28, s91, v232\n"
        "v_dot2c_f32_bf16 v29, v232, v232\n"
        "v_dot2c_f32_bf16 v28, s91, v233\n"
        "v_dot2c_f32_bf16 v29, v233, v233\n"
        "v_dot2c_f32_bf16 v28, s91, v234\n"
        "v_dot2c_f32_bf16 v29, v234, v234\n"
        "v_dot2c_f32_bf16 v28, s91, v235\n"
        "v_dot2c_f32_bf16 v29, v235, v235\n"
        "v_dot2c_f32_bf16 v28, s91, v236\n"
        "v_dot2c_f32_bf16 v29, v236, v236\n"
        "v_dot2c_f32_bf16 v28, s91, v237\n"
        "v_dot2c_f32_bf16 v29, v237, v237\n"
        "v_dot2c_f32_bf16 v28, s91, v238\n"
        "v_dot2c_f32_bf16 v29, v238, v238\n"
        "v_dot2c_f32_bf16 v28, s91, v239\n"
        "v_dot2c_f32_bf16 v29, v239, v239\n"
        "v_mfma_f32_16x16x32_bf16 a[100:103], a[96:99], a[0:3], a[100:103]\n"
        "buffer_load_dwordx4 v22, s[16:19], s60 offen lds\n"
        "s_bfe_u32 s61, s63, 0x40010\n"
        "s_add_u32 m0, s83, 0x5800\n"
        "v_mov_b32 v30, v28\n"
        "v_mfma_f32_16x16x32_bf16 a[104:107], a[96:99], a[4:7], a[104:107]\n"
        "s_mul_i32 s60, s61, 0x2a000\n"
        "v_mov_b32 v31, v29\n"
        "s_nop 0\n"
        "v_permlane32_swap_b32 v28, v30\n"
        "v_permlane32_swap_b32 v29, v31\n"
        "v_mfma_f32_32x32x16_bf16 v[48:63], a[0:3], v[224:227], v[48:63]\n"
        "v_add_f32 v28, v28, v30\n"
        "v_add_f32 v29, v29, v31\n"
        "v_mul_f32 v28, 0x3b124925, v28\n"
        "v_mul_f32 v29, 0x3b124925, v29\n"
        "v_fma_f32 v29, -v28, v28, v29\n"
        "v_max_f32 v29, 0, v29\n"
        "v_cvt_pk_bf16_f32 v30, v29, 0\n"
        "v_cvt_pk_bf16_f32 v31, -v28, -v28\n"
        "v_lshlrev_b32 v30, 16, v30\n"
        "v_mfma_f32_32x32x16_bf16 v[48:63], a[8:11], v[228:231], v[48:63]\n"
        "v_add_f32 v30, 0x3727c5ac, v30\n"
        "v_cvt_pk_bf16_f32 v30, v30, 0\n"
        "v_lshlrev_b32 v30, 16, v30\n"
        "v_sqrt_f32 v30, v30\n"
        "v_cndmask_b32 v112, v31, 0, s[92:93]\n"
        "v_cvt_pk_bf16_f32 v113, v30, 0\n"
        "v_lshlrev_b32 v30, 16, v113\n"
        "v_mfma_f32_32x32x16_bf16 v[48:63], a[16:19], v[232:235], v[48:63]\n"
        "ds_write_b32 v18, a100\n"
        "ds_write_b32 v18, a104 offset:40960\n"
        "buffer_load_dwordx4 v22, s[16:19], s60 offen lds\n"
        "s_bfe_u32 s61, s63, 0x40014\n"
        "s_add_u32 m0, s83, 0x5c00\n"
        "v_mfma_f32_32x32x16_bf16 v[48:63], a[24:27], v[236:239], v[48:63]\n"
        "s_mul_i32 s60, s61, 0x2a000\n"
        "v_rcp_f32 v26, v30\n"
        "s_nop 0\n"
        "v_mul_f32 v27, 0x3e8b97f6, v26\n"
        "s_waitcnt vmcnt(13) lgkmcnt(0)\n"
        "s_barrier\n"
        "v_mfma_f32_32x32x16_bf16 v[64:79], a[4:7], v[224:227], v[64:79]\n"
        "ds_read_b128 v[120:123], v19\n"
        "ds_read_b128 v[124:127], v19 offset:40960\n"
        "v_mfma_f32_32x32x16_bf16 v[64:79], a[12:15], v[228:231], v[64:79]\n"
        "ds_read_b64_tr_b16 a[64:65], v6 offset:4096\n"
        "ds_read_b64_tr_b16 a[66:67], v6 offset:4608\n"
        "ds_read_b64_tr_b16 a[68:69], v7 offset:4096\n"
        "ds_read_b64_tr_b16 a[70:71], v7 offset:4608\n"
        "v_mfma_f32_32x32x16_bf16 v[64:79], a[20:23], v[232:235], v[64:79]\n"
        "ds_read_b64_tr_b16 a[72:73], v8 offset:4096\n"
        "ds_read_b64_tr_b16 a[74:75], v8 offset:4608\n"
        "ds_read_b64_tr_b16 a[76:77], v9 offset:4096\n"
        "ds_read_b64_tr_b16 a[78:79], v9 offset:4608\n"
        "v_mfma_f32_32x32x16_bf16 v[64:79], a[28:31], v[236:239], v[64:79]\n"
        "s_waitcnt lgkmcnt(8)\n"
        "v_add_f32 v120, v120, v121\n"
        "v_add_f32 v122, v122, v123\n"
        "v_add_f32 v120, v120, v122\n"
        "v_cvt_pk_bf16_f32 v121, v120, 0\n"
        "v_lshlrev_b32 v121, 16, v121\n"
        "v_sub_f32 v121, v120, v121\n"
        "v_cvt_pk_bf16_f32 v116, v120, v121\n"
        "s_nop 0\n"
        "s_nop 0\n"
        "v_mfma_f32_32x32x16_bf16 v[48:63], v[116:119], v[112:115], v[48:63]\n"
        "v_add_f32 v124, v124, v125\n"
        "v_add_f32 v126, v126, v127\n"
        "v_add_f32 v124, v124, v126\n"
        "v_cvt_pk_bf16_f32 v125, v124, 0\n"
        "v_lshlrev_b32 v125, 16, v125\n"
        "v_sub_f32 v125, v124, v125\n"
        "v_cvt_pk_bf16_f32 v116, v124, v125\n"
        "s_nop 0\n"
        "s_nop 0\n"
        "v_mfma_f32_32x32x16_bf16 v[64:79], v[116:119], v[112:115], v[64:79]\n"
        "ds_read_b64_tr_b16 a[80:81], v6 offset:6144\n"
        "ds_read_b64_tr_b16 a[82:83], v6 offset:6656\n"
        "ds_read_b64_tr_b16 a[84:85], v7 offset:6144\n"
        "ds_read_b64_tr_b16 a[86:87], v7 offset:6656\n"
        "ds_read_b64_tr_b16 a[88:89], v8 offset:6144\n"
        "ds_read_b64_tr_b16 a[90:91], v8 offset:6656\n"
        "buffer_load_dwordx4 v22, s[16:19], s60 offen lds\n"
        "s_bfe_u32 s61, s63, 0x40018\n"
        "s_add_u32 m0, s83, 0x0\n"
        "s_mul_i32 s60, s61, 0x2a000\n"
        "buffer_load_dwordx4 v22, s[16:19], s60 offen lds\n"
        "s_waitcnt lgkmcnt(6)\n"
        "v_mfma_f32_32x32x16_bf16 v[96:111], a[64:67], v[128:131], 0\n"
        "ds_read_b64_tr_b16 a[92:93], v9 offset:6144\n"
        "ds_read_b64_tr_b16 a[94:95], v9 offset:6656\n"
        "v_mfma_f32_16x16x32_bf16 a[100:103], a[96:99], a[64:67], 0\n"
        "v_mul_f32 v48, v26, v48\n"
        "v_mul_f32 v49, v26, v49\n"
        "v_cvt_pk_bf16_f32 v80, v48, v49\n"
        "v_mul_f32 v50, v26, v50\n"
        "v_mul_f32 v51, v26, v51\n"
        "v_cvt_pk_bf16_f32 v81, v50, v51\n"
        "v_mfma_f32_32x32x16_bf16 v[96:111], a[68:71], v[132:135], v[96:111]\n"
        "ds_read_b64_tr_b16 a[64:65], v6 offset:8192\n"
        "ds_read_b64_tr_b16 a[66:67], v6 offset:8704\n"
        "v_mul_f32 v52, v26, v52\n"
        "v_mul_f32 v53, v26, v53\n"
        "v_cvt_pk_bf16_f32 v82, v52, v53\n"
        "v_mul_f32 v54, v26, v54\n"
        "v_mul_f32 v55, v26, v55\n"
        "v_cvt_pk_bf16_f32 v83, v54, v55\n"
        "v_mfma_f32_32x32x16_bf16 v[96:111], a[72:75], v[136:139], v[96:111]\n"
        "ds_read_b64_tr_b16 a[68:69], v7 offset:8192\n"
        "ds_read_b64_tr_b16 a[70:71], v7 offset:8704\n"
        "v_mul_f32 v56, v26, v56\n"
        "v_mul_f32 v57, v26, v57\n"
        "v_permlane32_swap_b32 v80, v82\n"
        "v_permlane32_swap_b32 v81, v83\n"
        "buffer_store_dwordx4 v[80:83], v1, s[32:35], s66 offen offset:0 sc1\n"
        "v_mfma_f32_32x32x16_bf16 v[96:111], a[76:79], v[140:143], v[96:111]\n"
        "ds_read_b64_tr_b16 a[72:73], v8 offset:8192\n"
        "ds_read_b64_tr_b16 a[74:75], v8 offset:8704\n"
        "v_cvt_pk_bf16_f32 v84, v56, v57\n"
        "v_mul_f32 v58, v26, v58\n"
        "v_mul_f32 v59, v26, v59\n"
        "v_cvt_pk_bf16_f32 v85, v58, v59\n"
        "v_mul_f32 v60, v26, v60\n"
        "v_mul_f32 v61, v26, v61\n"
        "s_waitcnt lgkmcnt(6)\n"
        "v_mfma_f32_32x32x16_bf16 v[96:111], a[80:83], v[144:147], v[96:111]\n"
        "ds_read_b64_tr_b16 a[76:77], v9 offset:8192\n"
        "ds_read_b64_tr_b16 a[78:79], v9 offset:8704\n"
        "v_cvt_pk_bf16_f32 v86, v60, v61\n"
        "v_mul_f32 v62, v26, v62\n"
        "v_mul_f32 v63, v26, v63\n"
        "v_cvt_pk_bf16_f32 v87, v62, v63\n"
        "v_mul_f32 v64, v26, v64\n"
        "v_mul_f32 v65, v26, v65\n"
        "v_mfma_f32_16x16x32_bf16 a[100:103], a[96:99], a[80:83], a[100:103]\n"
        "v_permlane32_swap_b32 v84, v86\n"
        "v_permlane32_swap_b32 v85, v87\n"
        "buffer_store_dwordx4 v[84:87], v1, s[32:35], s66 offen offset:1024 sc1\n"
        "v_cvt_pk_bf16_f32 v80, v64, v65\n"
        "v_mul_f32 v66, v26, v66\n"
        "v_mfma_f32_32x32x16_bf16 v[96:111], a[84:87], v[148:151], v[96:111]\n"
        "s_waitcnt vmcnt(9)\n"
        "s_barrier\n"
        "ds_read_b64_tr_b16 a[80:81], v6 offset:10240\n"
        "ds_read_b64_tr_b16 a[82:83], v6 offset:10752\n"
        "v_mul_f32 v67, v26, v67\n"
        "v_cvt_pk_bf16_f32 v81, v66, v67\n"
        "v_mfma_f32_32x32x16_bf16 v[96:111], a[88:91], v[152:155], v[96:111]\n"
        "ds_read_b64_tr_b16 a[84:85], v7 offset:10240\n"
        "ds_read_b64_tr_b16 a[86:87], v7 offset:10752\n"
        "v_mul_f32 v68, v26, v68\n"
        "v_mul_f32 v69, v26, v69\n"
        "v_cvt_pk_bf16_f32 v82, v68, v69\n"
        "v_mul_f32 v70, v26, v70\n"
        "v_mul_f32 v71, v26, v71\n"
        "v_cvt_pk_bf16_f32 v83, v70, v71\n"
        "v_mfma_f32_32x32x16_bf16 v[96:111], a[92:95], v[156:159], v[96:111]\n"
        "ds_read_b64_tr_b16 a[88:89], v8 offset:10240\n"
        "ds_read_b64_tr_b16 a[90:91], v8 offset:10752\n"
        "v_mul_f32 v72, v26, v72\n"
        "v_mul_f32 v73, v26, v73\n"
        "v_permlane32_swap_b32 v80, v82\n"
        "v_permlane32_swap_b32 v81, v83\n"
        "buffer_store_dwordx4 v[80:83], v1, s[32:35], s66 offen offset:2048 sc1\n"
        "s_waitcnt lgkmcnt(6)\n"
        "v_mfma_f32_32x32x16_bf16 v[96:111], a[64:67], v[160:163], v[96:111]\n"
        "ds_read_b64_tr_b16 a[92:93], v9 offset:10240\n"
        "ds_read_b64_tr_b16 a[94:95], v9 offset:10752\n"
        "v_cvt_pk_bf16_f32 v84, v72, v73\n"
        "v_mul_f32 v74, v26, v74\n"
        "v_mul_f32 v75, v26, v75\n"
        "v_cvt_pk_bf16_f32 v85, v74, v75\n"
        "v_mul_f32 v76, v26, v76\n"
        "v_mul_f32 v77, v26, v77\n"
        "v_mfma_f32_16x16x32_bf16 a[100:103], a[96:99], a[64:67], a[100:103]\n"
        "v_cvt_pk_bf16_f32 v86, v76, v77\n"
        "v_mul_f32 v78, v26, v78\n"
        "v_mul_f32 v79, v26, v79\n"
        "v_cvt_pk_bf16_f32 v87, v78, v79\n"
        "s_nop 1\n"
        "v_permlane32_swap_b32 v84, v86\n"
        "v_mfma_f32_32x32x16_bf16 v[96:111], a[68:71], v[164:167], v[96:111]\n"
        "ds_read_b64_tr_b16 a[64:65], v6 offset:12288\n"
        "ds_read_b64_tr_b16 a[66:67], v6 offset:12800\n"
        "v_permlane32_swap_b32 v85, v87\n"
        "buffer_store_dwordx4 v[84:87], v1, s[32:35], s66 offen offset:3072 sc1\n"
        "v_mfma_f32_32x32x16_bf16 v[96:111], a[72:75], v[168:171], v[96:111]\n"
        "ds_read_b64_tr_b16 a[68:69], v7 offset:12288\n"
        "ds_read_b64_tr_b16 a[70:71], v7 offset:12800\n"
        "v_mfma_f32_32x32x16_bf16 v[96:111], a[76:79], v[172:175], v[96:111]\n"
        "ds_read_b64_tr_b16 a[72:73], v8 offset:12288\n"
        "ds_read_b64_tr_b16 a[74:75], v8 offset:12800\n"
        "s_branch role_join_%=\n"
        "role_v_%=:\n"
        "s_sub_u32 s97, s81, 7\n"
        "s_lshl_b32 s97, s97, 6\n"
        "s_add_u32 s96, s66, 28672\n"
        "v_lshl_add_u32 v227, v238, 9, s96\n"
        "s_add_u32 s98, s97, 0\n"
        "v_add_u32 v224, s98, v237\n"
        "v_mul_u32_u24 v225, 2341, v224\n"
        "v_lshrrev_b32 v225, 16, v225\n"
        "v_mul_u32_u24 v226, 28, v225\n"
        "v_sub_u32 v224, v224, v226\n"
        "v_cmp_lt_u32 vcc, 23, v224\n"
        "v_lshl_add_u32 v248, v225, 11, v227\n"
        "v_lshl_add_u32 v248, v224, 4, v248\n"
        "v_mov_b32 v225, 0x40000000\n"
        "v_cndmask_b32 v230, v225, v248, vcc\n"
        "v_cmp_eq_u32 vcc, 24, v224\n"
        "v_mov_b32 v225, s91\n"
        "s_nop 0\n"
        "v_cndmask_b32 v216, 0, v225, vcc\n"
        "v_cndmask_b32 v217, 0, v225, vcc\n"
        "v_cndmask_b32 v218, 0, v225, vcc\n"
        "v_cndmask_b32 v219, 0, v225, vcc\n"
        "buffer_store_dwordx4 v[216:219], v230, s[32:35], 0 offen offset:64 sc1\n"
        "buffer_store_dwordx4 v[216:219], v230, s[32:35], 0 offen offset:1088 sc1\n"
        "s_add_u32 s98, s97, 32\n"
        "v_add_u32 v224, s98, v237\n"
        "v_mul_u32_u24 v225, 2341, v224\n"
        "v_lshrrev_b32 v225, 16, v225\n"
        "v_mul_u32_u24 v226, 28, v225\n"
        "v_sub_u32 v224, v224, v226\n"
        "v_cmp_lt_u32 vcc, 23, v224\n"
        "v_lshl_add_u32 v249, v225, 11, v227\n"
        "v_lshl_add_u32 v249, v224, 4, v249\n"
        "v_mov_b32 v225, 0x40000000\n"
        "v_cndmask_b32 v231, v225, v249, vcc\n"
        "v_cmp_eq_u32 vcc, 24, v224\n"
        "v_mov_b32 v225, s91\n"
        "s_nop 0\n"
        "v_cndmask_b32 v220, 0, v225, vcc\n"
        "v_cndmask_b32 v221, 0, v225, vcc\n"
        "v_cndmask_b32 v222, 0, v225, vcc\n"
        "v_cndmask_b32 v223, 0, v225, vcc\n"
        "buffer_store_dwordx4 v[220:223], v231, s[32:35], 0 offen offset:64 sc1\n"
        "buffer_store_dwordx4 v[220:223], v231, s[32:35], 0 offen offset:1088 sc1\n"
        "v_lshlrev_b32 v80, 2, v237\n"
        "v_add_u32 v80, s83, v80\n"
        "v_lshlrev_b32 v81, 4, v238\n"
        "v_add_u32 v81, s83, v81\n"
        "s_waitcnt vmcnt(26)\n"
        "s_barrier\n"
        "ds_read_b128 v[128:131], v2 offset:0\n"
        "ds_read_b128 v[132:135], v3 offset:0\n"
        "ds_read_b128 v[136:139], v4 offset:0\n"
        "ds_read_b128 v[140:143], v5 offset:0\n"
        "ds_read_b64_tr_b16 a[0:1], v6 offset:24576\n"
        "ds_read_b64_tr_b16 a[2:3], v6 offset:25088\n"
        "ds_read_b64_tr_b16 a[4:5], v10 offset:24576\n"
        "ds_read_b64_tr_b16 a[6:7], v10 offset:25088\n"
        "ds_read_b64_tr_b16 a[8:9], v7 offset:24576\n"
        "ds_read_b64_tr_b16 a[10:11], v7 offset:25088\n"
        "ds_read_b64_tr_b16 a[12:13], v11 offset:24576\n"
        "ds_read_b64_tr_b16 a[14:15], v11 offset:25088\n"
        "ds_read_b64_tr_b16 a[16:17], v8 offset:24576\n"
        "ds_read_b64_tr_b16 a[18:19], v8 offset:25088\n"
        "ds_read_b64_tr_b16 a[20:21], v12 offset:24576\n"
        "ds_read_b64_tr_b16 a[22:23], v12 offset:25088\n"
        "ds_read_b64_tr_b16 a[24:25], v9 offset:24576\n"
        "ds_read_b64_tr_b16 a[26:27], v9 offset:25088\n"
        "ds_read_b64_tr_b16 a[28:29], v13 offset:24576\n"
        "ds_read_b64_tr_b16 a[30:31], v13 offset:25088\n"
        "v_readfirstlane_b32 s88, v24\n"
        "v_readfirstlane_b32 s89, v25\n"
        "s_add_u32 s88, s88, 1\n"
        "s_addc_u32 s89, s89, 0\n"
        "v_mov_b32 v24, s88\n"
        "v_mov_b32 v25, s89\n"
        "s_waitcnt vmcnt(16) lgkmcnt(0)\n"
        "s_barrier\n"
        "v_mfma_f32_32x32x16_bf16 v[48:63], v[128:131], a[0:3], 0\n"
        "s_bfe_u32 s61, s63, 0x40010\n"
        "s_add_u32 m0, s83, 0x4000\n"
        "s_lshl_b32 s60, s61, 7\n"
        "buffer_load_dwordx4 v14, s[40:43], s60 offen lds\n"
        "ds_read_b128 v[144:147], v2 offset:4096\n"
        "v_mfma_f32_32x32x16_bf16 v[64:79], v[128:131], a[4:7], 0\n"
        "ds_read_b128 v[148:151], v3 offset:4096\n"
        "ds_read_b128 v[152:155], v4 offset:4096\n"
        "ds_read_b128 v[156:159], v5 offset:4096\n"
        "ds_read_b64_tr_b16 a[32:33], v6 offset:26624\n"
        "v_mfma_f32_16x16x32_bf16 a[100:103], a[96:99], a[0:3], 0\n"
        "ds_read_b64_tr_b16 a[34:35], v6 offset:27136\n"
        "buffer_load_dwordx4 v15, s[40:43], s60 offen offset:1024 lds\n"
        "v_mfma_f32_16x16x32_bf16 a[104:107], a[96:99], a[4:7], 0\n"
        "ds_read_b64_tr_b16 a[36:37], v10 offset:26624\n"
        "ds_read_b64_tr_b16 a[38:39], v10 offset:27136\n"
        "v_mfma_f32_32x32x16_bf16 v[48:63], v[132:135], a[8:11], v[48:63]\n"
        "ds_read_b64_tr_b16 a[40:41], v7 offset:26624\n"
        "ds_read_b64_tr_b16 a[42:43], v7 offset:27136\n"
        "ds_read_b64_tr_b16 a[44:45], v11 offset:26624\n"
        "ds_read_b64_tr_b16 a[46:47], v11 offset:27136\n"
        "v_mfma_f32_32x32x16_bf16 v[64:79], v[132:135], a[12:15], v[64:79]\n"
        "buffer_load_dwordx4 v16, s[40:43], s60 offen offset:2048 lds\n"
        "ds_read_b64_tr_b16 a[48:49], v8 offset:26624\n"
        "ds_read_b64_tr_b16 a[50:51], v8 offset:27136\n"
        "ds_read_b64_tr_b16 a[52:53], v12 offset:26624\n"
        "v_mfma_f32_32x32x16_bf16 v[48:63], v[136:139], a[16:19], v[48:63]\n"
        "ds_read_b64_tr_b16 a[54:55], v12 offset:27136\n"
        "ds_read_b64_tr_b16 a[56:57], v9 offset:26624\n"
        "ds_read_b64_tr_b16 a[58:59], v9 offset:27136\n"
        "ds_read_b64_tr_b16 a[60:61], v13 offset:26624\n"
        "v_mfma_f32_32x32x16_bf16 v[64:79], v[136:139], a[20:23], v[64:79]\n"
        "buffer_load_dwordx4 v17, s[40:43], s60 offen offset:3072 lds\n"
        "s_add_u32 m0, s83, 0x8000\n"
        "s_mul_i32 s60, s61, 0x2a000\n"
        "ds_read_b64_tr_b16 a[62:63], v13 offset:27136\n"
        "v_mfma_f32_32x32x16_bf16 v[48:63], v[140:143], a[24:27], v[48:63]\n"
        "v_mfma_f32_32x32x16_bf16 v[64:79], v[140:143], a[28:31], v[64:79]\n"
        "buffer_load_dwordx4 v18, s[12:15], s60 offen lds\n"
        "v_dot2c_f32_bf16 v28, s91, v128\n"
        "v_dot2c_f32_bf16 v29, v128, v128\n"
        "v_dot2c_f32_bf16 v28, s91, v129\n"
        "v_dot2c_f32_bf16 v29, v129, v129\n"
        "v_dot2c_f32_bf16 v28, s91, v130\n"
        "v_dot2c_f32_bf16 v29, v130, v130\n"
        "v_dot2c_f32_bf16 v28, s91, v131\n"
        "v_dot2c_f32_bf16 v29, v131, v131\n"
        "v_dot2c_f32_bf16 v28, s91, v132\n"
        "v_dot2c_f32_bf16 v29, v132, v132\n"
        "v_dot2c_f32_bf16 v28, s91, v133\n"
        "v_dot2c_f32_bf16 v29, v133, v133\n"
        "v_dot2c_f32_bf16 v28, s91, v134\n"
        "v_dot2c_f32_bf16 v29, v134, v134\n"
        "v_dot2c_f32_bf16 v28, s91, v135\n"
        "v_dot2c_f32_bf16 v29, v135, v135\n"
        "buffer_load_dwordx4 v19, s[12:15], s60 offen offset:1024 lds\n"
        "s_bfe_u32 s61, s63, 0x40014\n"
        "s_add_u32 m0, s83, 0x5000\n"
        "v_dot2c_f32_bf16 v28, s91, v136\n"
        "v_dot2c_f32_bf16 v29, v136, v136\n"
        "v_dot2c_f32_bf16 v28, s91, v137\n"
        "v_dot2c_f32_bf16 v29, v137, v137\n"
        "v_dot2c_f32_bf16 v28, s91, v138\n"
        "v_dot2c_f32_bf16 v29, v138, v138\n"
        "v_dot2c_f32_bf16 v28, s91, v139\n"
        "v_dot2c_f32_bf16 v29, v139, v139\n"
        "s_lshl_b32 s60, s61, 7\n"
        "v_dot2c_f32_bf16 v28, s91, v140\n"
        "v_dot2c_f32_bf16 v29, v140, v140\n"
        "v_dot2c_f32_bf16 v28, s91, v141\n"
        "v_dot2c_f32_bf16 v29, v141, v141\n"
        "v_dot2c_f32_bf16 v28, s91, v142\n"
        "v_dot2c_f32_bf16 v29, v142, v142\n"
        "v_dot2c_f32_bf16 v28, s91, v143\n"
        "v_dot2c_f32_bf16 v29, v143, v143\n"
        "buffer_load_dwordx4 v14, s[40:43], s60 offen lds\n"
        "s_waitcnt vmcnt(17) lgkmcnt(0)\n"
        "s_barrier\n"
        "v_mfma_f32_32x32x16_bf16 v[48:63], v[144:147], a[32:35], v[48:63]\n"
        "ds_read_b128 v[160:163], v2 offset:8192\n"
        "ds_read_b128 v[164:167], v3 offset:8192\n"
        "ds_read_b128 v[168:171], v4 offset:8192\n"
        "ds_read_b128 v[172:175], v5 offset:8192\n"
        "v_mfma_f32_32x32x16_bf16 v[64:79], v[144:147], a[36:39], v[64:79]\n"
        "buffer_load_dwordx4 v15, s[40:43], s60 offen offset:1024 lds\n"
        "ds_read_b64_tr_b16 a[0:1], v6 offset:28672\n"
        "ds_read_b64_tr_b16 a[2:3], v6 offset:29184\n"
        "ds_read_b64_tr_b16 a[4:5], v10 offset:28672\n"
        "v_mfma_f32_16x16x32_bf16 a[100:103], a[96:99], a[32:35], a[100:103]\n"
        "ds_read_b64_tr_b16 a[6:7], v10 offset:29184\n"
        "ds_read_b64_tr_b16 a[8:9], v7 offset:28672\n"
        "v_mfma_f32_16x16x32_bf16 a[104:107], a[96:99], a[36:39], a[104:107]\n"
        "ds_read_b64_tr_b16 a[10:11], v7 offset:29184\n"
        "buffer_load_dwordx4 v16, s[40:43], s60 offen offset:2048 lds\n"
        "v_mfma_f32_32x32x16_bf16 v[48:63], v[148:151], a[40:43], v[48:63]\n"
        "ds_read_b64_tr_b16 a[12:13], v11 offset:28672\n"
        "ds_read_b64_tr_b16 a[14:15], v11 offset:29184\n"
        "ds_read_b64_tr_b16 a[16:17], v8 offset:28672\n"
        "ds_read_b64_tr_b16 a[18:19], v8 offset:29184\n"
        "v_mfma_f32_32x32x16_bf16 v[64:79], v[148:151], a[44:47], v[64:79]\n"
        "ds_read_b64_tr_b16 a[20:21], v12 offset:28672\n"
        "ds_read_b64_tr_b16 a[22:23], v12 offset:29184\n"
        "buffer_load_dwordx4 v17, s[40:43], s60 offen offset:3072 lds\n"
        "s_add_u32 m0, s83, 0x8800\n"
        "s_mul_i32 s60, s61, 0x2a000\n"
        "v_mfma_f32_32x32x16_bf16 v[48:63], v[152:155], a[48:51], v[48:63]\n"
        "ds_read_b64_tr_b16 a[24:25], v9 offset:28672\n"
        "ds_read_b64_tr_b16 a[26:27], v9 offset:29184\n"
        "ds_read_b64_tr_b16 a[28:29], v13 offset:28672\n"
        "ds_read_b64_tr_b16 a[30:31], v13 offset:29184\n"
        "v_mfma_f32_32x32x16_bf16 v[64:79], v[152:155], a[52:55], v[64:79]\n"
        "v_mfma_f32_32x32x16_bf16 v[48:63], v[156:159], a[56:59], v[48:63]\n"
        "buffer_load_dwordx4 v18, s[12:15], s60 offen lds\n"
        "v_mfma_f32_32x32x16_bf16 v[64:79], v[156:159], a[60:63], v[64:79]\n"
        "v_dot2c_f32_bf16 v28, s91, v144\n"
        "v_dot2c_f32_bf16 v29, v144, v144\n"
        "v_dot2c_f32_bf16 v28, s91, v145\n"
        "v_dot2c_f32_bf16 v29, v145, v145\n"
        "v_dot2c_f32_bf16 v28, s91, v146\n"
        "v_dot2c_f32_bf16 v29, v146, v146\n"
        "v_dot2c_f32_bf16 v28, s91, v147\n"
        "v_dot2c_f32_bf16 v29, v147, v147\n"
        "buffer_load_dwordx4 v19, s[12:15], s60 offen offset:1024 lds\n"
        "s_bfe_u32 s61, s63, 0x40018\n"
        "s_add_u32 m0, s83, 0x0\n"
        "v_dot2c_f32_bf16 v28, s91, v148\n"
        "v_dot2c_f32_bf16 v29, v148, v148\n"
        "v_dot2c_f32_bf16 v28, s91, v149\n"
        "v_dot2c_f32_bf16 v29, v149, v149\n"
        "v_dot2c_f32_bf16 v28, s91, v150\n"
        "v_dot2c_f32_bf16 v29, v150, v150\n"
        "v_dot2c_f32_bf16 v28, s91, v151\n"
        "v_dot2c_f32_bf16 v29, v151, v151\n"
        "s_lshl_b32 s60, s61, 7\n"
        "v_dot2c_f32_bf16 v28, s91, v152\n"
        "v_dot2c_f32_bf16 v29, v152, v152\n"
        "v_dot2c_f32_bf16 v28, s91, v153\n"
        "v_dot2c_f32_bf16 v29, v153, v153\n"
        "v_dot2c_f32_bf16 v28, s91, v154\n"
        "v_dot2c_f32_bf16 v29, v154, v154\n"
        "v_dot2c_f32_bf16 v28, s91, v155\n"
        "v_dot2c_f32_bf16 v29, v155, v155\n"
        "buffer_load_dwordx4 v14, s[40:43], s60 offen lds\n"
        "v_dot2c_f32_bf16 v28, s91, v156\n"
        "v_dot2c_f32_bf16 v29, v156, v156\n"
        "v_dot2c_f32_bf16 v28, s91, v157\n"
        "v_dot2c_f32_bf16 v29, v157, v157\n"
        "v_dot2c_f32_bf16 v28, s91, v158\n"
        "v_dot2c_f32_bf16 v29, v158, v158\n"
        "v_dot2c_f32_bf16 v28, s91, v159\n"
        "v_dot2c_f32_bf16 v29, v159, v159\n"
        "s_waitcnt vmcnt(17) lgkmcnt(0)\n"
        "s_barrier\n"
        "v_mfma_f32_32x32x16_bf16 v[48:63], v[160:163], a[0:3], v[48:63]\n"
        "ds_read_b128 v[176:179], v2 offset:12288\n"
        "buffer_load_dwordx4 v15, s[40:43], s60 offen offset:1024 lds\n"
        "ds_read_b128 v[180:183], v3 offset:12288\n"
        "ds_read_b128 v[184:187], v4 offset:12288\n"
        "v_mfma_f32_32x32x16_bf16 v[64:79], v[160:163], a[4:7], v[64:79]\n"
        "ds_read_b128 v[188:191], v5 offset:12288\n"
        "ds_read_b64_tr_b16 a[32:33], v6 offset:30720\n"
        "ds_read_b64_tr_b16 a[34:35], v6 offset:31232\n"
        "ds_read_b64_tr_b16 a[36:37], v10 offset:30720\n"
        "v_mfma_f32_16x16x32_bf16 a[100:103], a[96:99], a[0:3], a[100:103]\n"
        "buffer_load_dwordx4 v16, s[40:43], s60 offen offset:2048 lds\n"
        "ds_read_b64_tr_b16 a[38:39], v10 offset:31232\n"
        "v_mfma_f32_16x16x32_bf16 a[104:107], a[96:99], a[4:7], a[104:107]\n"
        "ds_read_b64_tr_b16 a[40:41], v7 offset:30720\n"
        "ds_read_b64_tr_b16 a[42:43], v7 offset:31232\n"
        "v_mfma_f32_32x32x16_bf16 v[48:63], v[164:167], a[8:11], v[48:63]\n"
        "ds_read_b64_tr_b16 a[44:45], v11 offset:30720\n"
        "ds_read_b64_tr_b16 a[46:47], v11 offset:31232\n"
        "ds_read_b64_tr_b16 a[48:49], v8 offset:30720\n"
        "buffer_load_dwordx4 v17, s[40:43], s60 offen offset:3072 lds\n"
        "v_mfma_f32_32x32x16_bf16 v[64:79], v[164:167], a[12:15], v[64:79]\n"
        "s_add_u32 m0, s83, 0x9000\n"
        "s_mul_i32 s60, s61, 0x2a000\n"
        "ds_read_b64_tr_b16 a[50:51], v8 offset:31232\n"
        "ds_read_b64_tr_b16 a[52:53], v12 offset:30720\n"
        "ds_read_b64_tr_b16 a[54:55], v12 offset:31232\n"
        "v_mfma_f32_32x32x16_bf16 v[48:63], v[168:171], a[16:19], v[48:63]\n"
        "ds_read_b64_tr_b16 a[56:57], v9 offset:30720\n"
        "ds_read_b64_tr_b16 a[58:59], v9 offset:31232\n"
        "buffer_load_dwordx4 v18, s[12:15], s60 offen lds\n"
        "ds_read_b64_tr_b16 a[60:61], v13 offset:30720\n"
        "v_mfma_f32_32x32x16_bf16 v[64:79], v[168:171], a[20:23], v[64:79]\n"
        "ds_read_b64_tr_b16 a[62:63], v13 offset:31232\n"
        "v_mfma_f32_32x32x16_bf16 v[48:63], v[172:175], a[24:27], v[48:63]\n"
        "v_mfma_f32_32x32x16_bf16 v[64:79], v[172:175], a[28:31], v[64:79]\n"
        "buffer_load_dwordx4 v19, s[12:15], s60 offen offset:1024 lds\n"
        "s_bfe_u32 s61, s63, 0x40000\n"
        "s_add_u32 m0, s83, 0x1000\n"
        "s_mul_i32 s60, s61, 0x38000\n"
        "v_dot2c_f32_bf16 v28, s91, v160\n"
        "v_dot2c_f32_bf16 v29, v160, v160\n"
        "v_dot2c_f32_bf16 v28, s91, v161\n"
        "v_dot2c_f32_bf16 v29, v161, v161\n"
        "v_dot2c_f32_bf16 v28, s91, v162\n"
        "v_dot2c_f32_bf16 v29, v162, v162\n"
        "v_dot2c_f32_bf16 v28, s91, v163\n"
        "v_dot2c_f32_bf16 v29, v163, v163\n"
        "v_dot2c_f32_bf16 v28, s91, v164\n"
        "v_dot2c_f32_bf16 v29, v164, v164\n"
        "v_dot2c_f32_bf16 v28, s91, v165\n"
        "v_dot2c_f32_bf16 v29, v165, v165\n"
        "v_dot2c_f32_bf16 v28, s91, v166\n"
        "v_dot2c_f32_bf16 v29, v166, v166\n"
        "v_dot2c_f32_bf16 v28, s91, v167\n"
        "v_dot2c_f32_bf16 v29, v167, v167\n"
        "buffer_load_dwordx4 v20, s[20:23], s60 offen lds\n"
        "v_dot2c_f32_bf16 v28, s91, v168\n"
        "v_dot2c_f32_bf16 v29, v168, v168\n"
        "v_dot2c_f32_bf16 v28, s91, v169\n"
        "v_dot2c_f32_bf16 v29, v169, v169\n"
        "v_dot2c_f32_bf16 v28, s91, v170\n"
        "v_dot2c_f32_bf16 v29, v170, v170\n"
        "v_dot2c_f32_bf16 v28, s91, v171\n"
        "v_dot2c_f32_bf16 v29, v171, v171\n"
        "v_dot2c_f32_bf16 v28, s91, v172\n"
        "v_dot2c_f32_bf16 v29, v172, v172\n"
        "v_dot2c_f32_bf16 v28, s91, v173\n"
        "v_dot2c_f32_bf16 v29, v173, v173\n"
        "v_dot2c_f32_bf16 v28, s91, v174\n"
        "v_dot2c_f32_bf16 v29, v174, v174\n"
        "v_dot2c_f32_bf16 v28, s91, v175\n"
        "v_dot2c_f32_bf16 v29, v175, v175\n"
        "buffer_load_dwordx4 v21, s[20:23], s60 offen offset:1024 lds\n"
        "s_bfe_u32 s61, s63, 0x40004\n"
        "s_add_u32 m0, s83, 0x1800\n"
        "s_waitcnt vmcnt(14) lgkmcnt(0)\n"
        "s_barrier\n"
        "v_mfma_f32_32x32x16_bf16 v[48:63], v[176:179], a[32:35], v[48:63]\n"
        "s_mul_i32 s60, s61, 0x38000\n"
        "ds_read_b128 v[192:195], v2 offset:16384\n"
        "ds_read_b128 v[196:199], v3 offset:16384\n"
        "ds_read_b128 v[200:203], v4 offset:16384\n"
        "v_mfma_f32_32x32x16_bf16 v[64:79], v[176:179], a[36:39], v[64:79]\n"
        "buffer_load_dwordx4 v20, s[20:23], s60 offen lds\n"
        "ds_read_b128 v[204:207], v5 offset:16384\n"
        "ds_read_b64_tr_b16 a[0:1], v6 offset:32768\n"
        "ds_read_b64_tr_b16 a[2:3], v6 offset:33280\n"
        "v_mfma_f32_16x16x32_bf16 a[100:103], a[96:99], a[32:35], a[100:103]\n"
        "ds_read_b64_tr_b16 a[4:5], v10 offset:32768\n"
        "ds_read_b64_tr_b16 a[6:7], v10 offset:33280\n"
        "v_mfma_f32_16x16x32_bf16 a[104:107], a[96:99], a[36:39], a[104:107]\n"
        "ds_read_b64_tr_b16 a[8:9], v7 offset:32768\n"
        "buffer_load_dwordx4 v21, s[20:23], s60 offen offset:1024 lds\n"
        "v_mfma_f32_32x32x16_bf16 v[48:63], v[180:183], a[40:43], v[48:63]\n"
        "s_bfe_u32 s61, s63, 0x40008\n"
        "s_add_u32 m0, s83, 0x2000\n"
        "s_mul_i32 s60, s61, 0x38000\n"
        "ds_read_b64_tr_b16 a[10:11], v7 offset:33280\n"
        "ds_read_b64_tr_b16 a[12:13], v11 offset:32768\n"
        "v_mfma_f32_32x32x16_bf16 v[64:79], v[180:183], a[44:47], v[64:79]\n"
        "ds_read_b64_tr_b16 a[14:15], v11 offset:33280\n"
        "ds_read_b64_tr_b16 a[16:17], v8 offset:32768\n"
        "ds_read_b64_tr_b16 a[18:19], v8 offset:33280\n"
        "buffer_load_dwordx4 v20, s[20:23], s60 offen lds\n"
        "v_mfma_f32_32x32x16_bf16 v[48:63], v[184:187], a[48:51], v[48:63]\n"
        "ds_read_b64_tr_b16 a[20:21], v12 offset:32768\n"
        "ds_read_b64_tr_b16 a[22:23], v12 offset:33280\n"
        "ds_read_b64_tr_b16 a[24:25], v9 offset:32768\n"
        "ds_read_b64_tr_b16 a[26:27], v9 offset:33280\n"
        "v_mfma_f32_32x32x16_bf16 v[64:79], v[184:187], a[52:55], v[64:79]\n"
        "ds_read_b64_tr_b16 a[28:29], v13 offset:32768\n"
        "ds_read_b64_tr_b16 a[30:31], v13 offset:33280\n"
        "buffer_load_dwordx4 v21, s[20:23], s60 offen offset:1024 lds\n"
        "s_bfe_u32 s61, s63, 0x4000c\n"
        "s_add_u32 m0, s83, 0x2800\n"
        "v_mfma_f32_32x32x16_bf16 v[48:63], v[188:191], a[56:59], v[48:63]\n"
        "s_mul_i32 s60, s61, 0x38000\n"
        "v_mfma_f32_32x32x16_bf16 v[64:79], v[188:191], a[60:63], v[64:79]\n"
        "v_dot2c_f32_bf16 v28, s91, v176\n"
        "v_dot2c_f32_bf16 v29, v176, v176\n"
        "v_dot2c_f32_bf16 v28, s91, v177\n"
        "v_dot2c_f32_bf16 v29, v177, v177\n"
        "v_dot2c_f32_bf16 v28, s91, v178\n"
        "v_dot2c_f32_bf16 v29, v178, v178\n"
        "v_dot2c_f32_bf16 v28, s91, v179\n"
        "v_dot2c_f32_bf16 v29, v179, v179\n"
        "buffer_load_dwordx4 v20, s[20:23], s60 offen lds\n"
        "v_dot2c_f32_bf16 v28, s91, v180\n"
        "v_dot2c_f32_bf16 v29, v180, v180\n"
        "v_dot2c_f32_bf16 v28, s91, v181\n"
        "v_dot2c_f32_bf16 v29, v181, v181\n"
        "v_dot2c_f32_bf16 v28, s91, v182\n"
        "v_dot2c_f32_bf16 v29, v182, v182\n"
        "v_dot2c_f32_bf16 v28, s91, v183\n"
        "v_dot2c_f32_bf16 v29, v183, v183\n"
        "v_dot2c_f32_bf16 v28, s91, v184\n"
        "v_dot2c_f32_bf16 v29, v184, v184\n"
        "v_dot2c_f32_bf16 v28, s91, v185\n"
        "v_dot2c_f32_bf16 v29, v185, v185\n"
        "v_dot2c_f32_bf16 v28, s91, v186\n"
        "v_dot2c_f32_bf16 v29, v186, v186\n"
        "v_dot2c_f32_bf16 v28, s91, v187\n"
        "v_dot2c_f32_bf16 v29, v187, v187\n"
        "buffer_load_dwordx4 v21, s[20:23], s60 offen offset:1024 lds\n"
        "s_bfe_u32 s61, s63, 0x40010\n"
        "s_add_u32 m0, s83, 0x3000\n"
        "v_dot2c_f32_bf16 v28, s91, v188\n"
        "v_dot2c_f32_bf16 v29, v188, v188\n"
        "v_dot2c_f32_bf16 v28, s91, v189\n"
        "v_dot2c_f32_bf16 v29, v189, v189\n"
        "v_dot2c_f32_bf16 v28, s91, v190\n"
        "v_dot2c_f32_bf16 v29, v190, v190\n"
        "v_dot2c_f32_bf16 v28, s91, v191\n"
        "v_dot2c_f32_bf16 v29, v191, v191\n"
        "s_mul_i32 s60, s61, 0x38000\n"
        "s_waitcnt vmcnt(14) lgkmcnt(0)\n"
        "s_barrier\n"
        "v_mfma_f32_32x32x16_bf16 v[48:63], v[192:195], a[0:3], v[48:63]\n"
        "buffer_load_dwordx4 v20, s[20:23], s60 offen lds\n"
        "ds_read_b128 v[208:211], v2 offset:20480\n"
        "ds_read_b128 v[212:215], v3 offset:20480\n"
        "ds_read_b128 v[216:219], v4 offset:20480\n"
        "v_mfma_f32_32x32x16_bf16 v[64:79], v[192:195], a[4:7], v[64:79]\n"
        "ds_read_b128 v[220:223], v5 offset:20480\n"
        "ds_read_b64_tr_b16 a[32:33], v6 offset:34816\n"
        "ds_read_b64_tr_b16 a[34:35], v6 offset:35328\n"
        "ds_read_b64_tr_b16 a[36:37], v10 offset:34816\n"
        "v_mfma_f32_16x16x32_bf16 a[100:103], a[96:99], a[0:3], a[100:103]\n"
        "buffer_load_dwordx4 v21, s[20:23], s60 offen offset:1024 lds\n"
        "s_bfe_u32 s61, s63, 0x40014\n"
        "s_add_u32 m0, s83, 0x3800\n"
        "v_mfma_f32_16x16x32_bf16 a[104:107], a[96:99], a[4:7], a[104:107]\n"
        "s_mul_i32 s60, s61, 0x38000\n"
        "ds_read_b64_tr_b16 a[38:39], v10 offset:35328\n"
        "v_mfma_f32_32x32x16_bf16 v[48:63], v[196:199], a[8:11], v[48:63]\n"
        "ds_read_b64_tr_b16 a[40:41], v7 offset:34816\n"
        "ds_read_b64_tr_b16 a[42:43], v7 offset:35328\n"
        "ds_read_b64_tr_b16 a[44:45], v11 offset:34816\n"
        "ds_read_b64_tr_b16 a[46:47], v11 offset:35328\n"
        "v_mfma_f32_32x32x16_bf16 v[64:79], v[196:199], a[12:15], v[64:79]\n"
        "buffer_load_dwordx4 v20, s[20:23], s60 offen lds\n"
        "ds_read_b64_tr_b16 a[48:49], v8 offset:34816\n"
        "ds_read_b64_tr_b16 a[50:51], v8 offset:35328\n"
        "ds_read_b64_tr_b16 a[52:53], v12 offset:34816\n"
        "v_mfma_f32_32x32x16_bf16 v[48:63], v[200:203], a[16:19], v[48:63]\n"
        "ds_read_b64_tr_b16 a[54:55], v12 offset:35328\n"
        "ds_read_b64_tr_b16 a[56:57], v9 offset:34816\n"
        "ds_read_b64_tr_b16 a[58:59], v9 offset:35328\n"
        "ds_read_b64_tr_b16 a[60:61], v13 offset:34816\n"
        "v_mfma_f32_32x32x16_bf16 v[64:79], v[200:203], a[20:23], v[64:79]\n"
        "buffer_load_dwordx4 v21, s[20:23], s60 offen offset:1024 lds\n"
        "s_bfe_u32 s61, s63, 0x40018\n"
        "s_add_u32 m0, s83, 0x4000\n"
        "s_mul_i32 s60, s61, 0x38000\n"
        "ds_read_b64_tr_b16 a[62:63], v13 offset:35328\n"
        "v_mfma_f32_32x32x16_bf16 v[48:63], v[204:207], a[24:27], v[48:63]\n"
        "v_add_u32 v19, %3, v1\n"
        "v_add_u32 v19, 38912, v19\n"
        "s_lshl_b32 s96, s78, 2\n"
        "v_add_u32 v18, s96, v19\n"
        "v_mfma_f32_32x32x16_bf16 v[64:79], v[204:207], a[28:31], v[64:79]\n"
        "buffer_load_dwordx4 v20, s[20:23], s60 offen lds\n"
        "v_dot2c_f32_bf16 v28, s91, v192\n"
        "v_dot2c_f32_bf16 v29, v192, v192\n"
        "v_dot2c_f32_bf16 v28, s91, v193\n"
        "v_dot2c_f32_bf16 v29, v193, v193\n"
        "v_dot2c_f32_bf16 v28, s91, v194\n"
        "v_dot2c_f32_bf16 v29, v194, v194\n"
        "v_dot2c_f32_bf16 v28, s91, v195\n"
        "v_dot2c_f32_bf16 v29, v195, v195\n"
        "v_dot2c_f32_bf16 v28, s91, v196\n"
        "v_dot2c_f32_bf16 v29, v196, v196\n"
        "v_dot2c_f32_bf16 v28, s91, v197\n"
        "v_dot2c_f32_bf16 v29, v197, v197\n"
        "v_dot2c_f32_bf16 v28, s91, v198\n"
        "v_dot2c_f32_bf16 v29, v198, v198\n"
        "v_dot2c_f32_bf16 v28, s91, v199\n"
        "v_dot2c_f32_bf16 v29, v199, v199\n"
        "buffer_load_dwordx4 v21, s[20:23], s60 offen offset:1024 lds\n"
        "s_bfe_u32 s61, s63, 0x40000\n"
        "s_add_u32 m0, s83, 0x4800\n"
        "v_dot2c_f32_bf16 v28, s91, v200\n"
        "v_dot2c_f32_bf16 v29, v200, v200\n"
        "v_dot2c_f32_bf16 v28, s91, v201\n"
        "v_dot2c_f32_bf16 v29, v201, v201\n"
        "v_dot2c_f32_bf16 v28, s91, v202\n"
        "v_dot2c_f32_bf16 v29, v202, v202\n"
        "v_dot2c_f32_bf16 v28, s91, v203\n"
        "v_dot2c_f32_bf16 v29, v203, v203\n"
        "s_mul_i32 s60, s61, 0x2a000\n"
        "v_dot2c_f32_bf16 v28, s91, v204\n"
        "v_dot2c_f32_bf16 v29, v204, v204\n"
        "v_dot2c_f32_bf16 v28, s91, v205\n"
        "v_dot2c_f32_bf16 v29, v205, v205\n"
        "v_dot2c_f32_bf16 v28, s91, v206\n"
        "v_dot2c_f32_bf16 v29, v206, v206\n"
        "v_dot2c_f32_bf16 v28, s91, v207\n"
        "v_dot2c_f32_bf16 v29, v207, v207\n"
        "s_waitcnt vmcnt(14) lgkmcnt(0)\n"
        "s_barrier\n"
        "v_mfma_f32_32x32x16_bf16 v[48:63], v[208:211], a[32:35], v[48:63]\n"
        "ds_read_b128 v[224:227], v2 offset:0\n"
        "ds_read_b128 v[228:231], v3 offset:0\n"
        "ds_read_b128 v[232:235], v4 offset:0\n"
        "ds_read_b128 v[236:239], v5 offset:0\n"
        "v_mfma_f32_32x32x16_bf16 v[64:79], v[208:211], a[36:39], v[64:79]\n"
        "buffer_load_dwordx4 v22, s[16:19], s60 offen lds\n"
        "s_bfe_u32 s61, s63, 0x40004\n"
        "s_add_u32 m0, s83, 0x4c00\n"
        "s_mul_i32 s60, s61, 0x2a000\n"
        "ds_read_b64_tr_b16 a[0:1], v6 offset:36864\n"
        "v_mfma_f32_16x16x32_bf16 a[100:103], a[96:99], a[32:35], a[100:103]\n"
        "ds_read_b64_tr_b16 a[2:3], v6 offset:37376\n"
        "ds_read_b64_tr_b16 a[4:5], v10 offset:36864\n"
        "v_mfma_f32_16x16x32_bf16 a[104:107], a[96:99], a[36:39], a[104:107]\n"
        "ds_read_b64_tr_b16 a[6:7], v10 offset:37376\n"
        "ds_read_b64_tr_b16 a[8:9], v7 offset:36864\n"
        "v_mfma_f32_32x32x16_bf16 v[48:63], v[212:215], a[40:43], v[48:63]\n"
        "ds_read_b64_tr_b16 a[10:11], v7 offset:37376\n"
        "ds_read_b64_tr_b16 a[12:13], v11 offset:36864\n"
        "ds_read_b64_tr_b16 a[14:15], v11 offset:37376\n"
        "ds_read_b64_tr_b16 a[16:17], v8 offset:36864\n"
        "v_mfma_f32_32x32x16_bf16 v[64:79], v[212:215], a[44:47], v[64:79]\n"
        "ds_read_b64_tr_b16 a[18:19], v8 offset:37376\n"
        "ds_read_b64_tr_b16 a[20:21], v12 offset:36864\n"
        "ds_read_b64_tr_b16 a[22:23], v12 offset:37376\n"
        "ds_read_b64_tr_b16 a[24:25], v9 offset:36864\n"
        "v_mfma_f32_32x32x16_bf16 v[48:63], v[216:219], a[48:51], v[48:63]\n"
        "buffer_load_dwordx4 v22, s[16:19], s60 offen lds\n"
        "s_bfe_u32 s61, s63, 0x40008\n"
        "s_add_u32 m0, s83, 0x5000\n"
        "s_mul_i32 s60, s61, 0x2a000\n"
        "ds_read_b64_tr_b16 a[26:27], v9 offset:37376\n"
        "v_mfma_f32_32x32x16_bf16 v[64:79], v[216:219], a[52:55], v[64:79]\n"
        "ds_read_b64_tr_b16 a[28:29], v13 offset:36864\n"
        "ds_read_b64_tr_b16 a[30:31], v13 offset:37376\n"
        "v_mfma_f32_32x32x16_bf16 v[48:63], v[220:223], a[56:59], v[48:63]\n"
        "v_mfma_f32_32x32x16_bf16 v[64:79], v[220:223], a[60:63], v[64:79]\n"
        "v_dot2c_f32_bf16 v28, s91, v208\n"
        "v_dot2c_f32_bf16 v29, v208, v208\n"
        "v_dot2c_f32_bf16 v28, s91, v209\n"
        "v_dot2c_f32_bf16 v29, v209, v209\n"
        "v_dot2c_f32_bf16 v28, s91, v210\n"
        "v_dot2c_f32_bf16 v29, v210, v210\n"
        "v_dot2c_f32_bf16 v28, s91, v211\n"
        "v_dot2c_f32_bf16 v29, v211, v211\n"
        "v_dot2c_f32_bf16 v28, s91, v212\n"
        "v_dot2c_f32_bf16 v29, v212, v212\n"
        "v_dot2c_f32_bf16 v28, s91, v213\n"
        "v_dot2c_f32_bf16 v29, v213, v213\n"
        "v_dot2c_f32_bf16 v28, s91, v214\n"
        "v_dot2c_f32_bf16 v29, v214, v214\n"
        "v_dot2c_f32_bf16 v28, s91, v215\n"
        "v_dot2c_f32_bf16 v29, v215, v215\n"
        "buffer_load_dwordx4 v22, s[16:19], s60 offen lds\n"
        "s_bfe_u32 s61, s63, 0x4000c\n"
        "s_add_u32 m0, s83, 0x5400\n"
        "v_dot2c_f32_bf16 v28, s91, v216\n"
        "v_dot2c_f32_bf16 v29, v216, v216\n"
        "v_dot2c_f32_bf16 v28, s91, v217\n"
        "v_dot2c_f32_bf16 v29, v217, v217\n"
        "v_dot2c_f32_bf16 v28, s91, v218\n"
        "v_dot2c_f32_bf16 v29, v218, v218\n"
        "v_dot2c_f32_bf16 v28, s91, v219\n"
        "v_dot2c_f32_bf16 v29, v219, v219\n"
        "s_mul_i32 s60, s61, 0x2a000\n"
        "v_dot2c_f32_bf16 v28, s91, v220\n"
        "v_dot2c_f32_bf16 v29, v220, v220\n"
        "v_dot2c_f32_bf16 v28, s91, v221\n"
        "v_dot2c_f32_bf16 v29, v221, v221\n"
        "v_dot2c_f32_bf16 v28, s91, v222\n"
        "v_dot2c_f32_bf16 v29, v222, v222\n"
        "v_dot2c_f32_bf16 v28, s91, v223\n"
        "v_dot2c_f32_bf16 v29, v223, v223\n"
        "s_waitcnt lgkmcnt(0)\n"
        "v_dot2c_f32_bf16 v28, s91, v224\n"
        "v_dot2c_f32_bf16 v29, v224, v224\n"
        "v_dot2c_f32_bf16 v28, s91, v225\n"
        "v_dot2c_f32_bf16 v29, v225, v225\n"
        "v_dot2c_f32_bf16 v28, s91, v226\n"
        "v_dot2c_f32_bf16 v29, v226, v226\n"
        "v_dot2c_f32_bf16 v28, s91, v227\n"
        "v_dot2c_f32_bf16 v29, v227, v227\n"
        "v_dot2c_f32_bf16 v28, s91, v228\n"
        "v_dot2c_f32_bf16 v29, v228, v228\n"
        "v_dot2c_f32_bf16 v28, s91, v229\n"
        "v_dot2c_f32_bf16 v29, v229, v229\n"
        "v_dot2c_f32_bf16 v28, s91, v230\n"
        "v_dot2c_f32_bf16 v29, v230, v230\n"
        "v_dot2c_f32_bf16 v28, s91, v231\n"
        "v_dot2c_f32_bf16 v29, v231, v231\n"
        "v_dot2c_f32_bf16 v28, s91, v232\n"
        "v_dot2c_f32_bf16 v29, v232, v232\n"
        "v_dot2c_f32_bf16 v28, s91, v233\n"
        "v_dot2c_f32_bf16 v29, v233, v233\n"
        "v_dot2c_f32_bf16 v28, s91, v234\n"
        "v_dot2c_f32_bf16 v29, v234, v234\n"
        "v_dot2c_f32_bf16 v28, s91, v235\n"
        "v_dot2c_f32_bf16 v29, v235, v235\n"
        "v_dot2c_f32_bf16 v28, s91, v236\n"
        "v_dot2c_f32_bf16 v29, v236, v236\n"
        "v_dot2c_f32_bf16 v28, s91, v237\n"
        "v_dot2c_f32_bf16 v29, v237, v237\n"
        "v_dot2c_f32_bf16 v28, s91, v238\n"
        "v_dot2c_f32_bf16 v29, v238, v238\n"
        "v_dot2c_f32_bf16 v28, s91, v239\n"
        "v_dot2c_f32_bf16 v29, v239, v239\n"
        "v_mfma_f32_16x16x32_bf16 a[100:103], a[96:99], a[0:3], a[100:103]\n"
        "buffer_load_dwordx4 v22, s[16:19], s60 offen lds\n"
        "s_bfe_u32 s61, s63, 0x40010\n"
        "s_add_u32 m0, s83, 0x5800\n"
        "v_mov_b32 v30, v28\n"
        "v_mfma_f32_16x16x32_bf16 a[104:107], a[96:99], a[4:7], a[104:107]\n"
        "s_mul_i32 s60, s61, 0x2a000\n"
        "v_mov_b32 v31, v29\n"
        "s_nop 0\n"
        "v_permlane32_swap_b32 v28, v30\n"
        "v_permlane32_swap_b32 v29, v31\n"
        "v_mfma_f32_32x32x16_bf16 v[48:63], v[224:227], a[0:3], v[48:63]\n"
        "v_add_f32 v28, v28, v30\n"
        "v_add_f32 v29, v29, v31\n"
        "v_mul_f32 v28, 0x3b124925, v28\n"
        "v_mul_f32 v29, 0x3b124925, v29\n"
        "v_fma_f32 v29, -v28, v28, v29\n"
        "v_max_f32 v29, 0, v29\n"
        "v_cvt_pk_bf16_f32 v30, v29, 0\n"
        "v_cvt_pk_bf16_f32 v31, -v28, -v28\n"
        "v_lshlrev_b32 v30, 16, v30\n"
        "v_mfma_f32_32x32x16_bf16 v[48:63], v[228:231], a[8:11], v[48:63]\n"
        "v_add_f32 v30, 0x3727c5ac, v30\n"
        "v_cvt_pk_bf16_f32 v30, v30, 0\n"
        "v_lshlrev_b32 v30, 16, v30\n"
        "v_sqrt_f32 v30, v30\n"
        "v_cndmask_b32 v112, v31, 0, s[92:93]\n"
        "v_cvt_pk_bf16_f32 v113, v30, 0\n"
        "v_lshlrev_b32 v30, 16, v113\n"
        "v_mfma_f32_32x32x16_bf16 v[48:63], v[232:235], a[16:19], v[48:63]\n"
        "ds_write_b32 v18, a100\n"
        "ds_write_b32 v18, a104 offset:40960\n"
        "buffer_load_dwordx4 v22, s[16:19], s60 offen lds\n"
        "s_bfe_u32 s61, s63, 0x40014\n"
        "s_add_u32 m0, s83, 0x5c00\n"
        "v_mfma_f32_32x32x16_bf16 v[48:63], v[236:239], a[24:27], v[48:63]\n"
        "s_mul_i32 s60, s61, 0x2a000\n"
        "v_rcp_f32 v26, v30\n"
        "ds_write_b32 v80, v26 offset:39936\n"
        "ds_read_b128 v[32:35], v81 offset:39936\n"
        "ds_read_b128 v[36:39], v81 offset:39968\n"
        "s_waitcnt vmcnt(13) lgkmcnt(3)\n"
        "s_barrier\n"
        "v_mfma_f32_32x32x16_bf16 v[64:79], v[224:227], a[4:7], v[64:79]\n"
        "ds_read_b128 v[120:123], v19\n"
        "ds_read_b128 v[124:127], v19 offset:40960\n"
        "ds_read_b128 v[40:43], v81 offset:40000\n"
        "ds_read_b128 v[44:47], v81 offset:40032\n"
        "v_mul_f32 v27, 0x3e8b97f6, v26\n"
        "v_mfma_f32_32x32x16_bf16 v[64:79], v[228:231], a[12:15], v[64:79]\n"
        "ds_read_b64_tr_b16 a[64:65], v6 offset:4096\n"
        "ds_read_b64_tr_b16 a[66:67], v6 offset:4608\n"
        "ds_read_b64_tr_b16 a[68:69], v7 offset:4096\n"
        "ds_read_b64_tr_b16 a[70:71], v7 offset:4608\n"
        "v_mfma_f32_32x32x16_bf16 v[64:79], v[232:235], a[20:23], v[64:79]\n"
        "ds_read_b64_tr_b16 a[72:73], v8 offset:4096\n"
        "ds_read_b64_tr_b16 a[74:75], v8 offset:4608\n"
        "ds_read_b64_tr_b16 a[76:77], v9 offset:4096\n"
        "ds_read_b64_tr_b16 a[78:79], v9 offset:4608\n"
        "v_mfma_f32_32x32x16_bf16 v[64:79], v[236:239], a[28:31], v[64:79]\n"
        "s_waitcnt lgkmcnt(10)\n"
        "v_add_f32 v120, v120, v121\n"
        "v_add_f32 v122, v122, v123\n"
        "v_add_f32 v120, v120, v122\n"
        "v_cvt_pk_bf16_f32 v121, v120, 0\n"
        "v_lshlrev_b32 v121, 16, v121\n"
        "v_sub_f32 v121, v120, v121\n"
        "v_cvt_pk_bf16_f32 v116, v120, v121\n"
        "s_nop 0\n"
        "s_nop 0\n"
        "v_mfma_f32_32x32x16_bf16 v[48:63], v[112:115], v[116:119], v[48:63]\n"
        "v_add_f32 v124, v124, v125\n"
        "v_add_f32 v126, v126, v127\n"
        "v_add_f32 v124, v124, v126\n"
        "v_cvt_pk_bf16_f32 v125, v124, 0\n"
        "v_lshlrev_b32 v125, 16, v125\n"
        "v_sub_f32 v125, v124, v125\n"
        "v_cvt_pk_bf16_f32 v116, v124, v125\n"
        "s_nop 0\n"
        "s_nop 0\n"
        "v_mfma_f32_32x32x16_bf16 v[64:79], v[112:115], v[116:119], v[64:79]\n"
        "ds_read_b64_tr_b16 a[80:81], v6 offset:6144\n"
        "ds_read_b64_tr_b16 a[82:83], v6 offset:6656\n"
        "ds_read_b64_tr_b16 a[84:85], v7 offset:6144\n"
        "ds_read_b64_tr_b16 a[86:87], v7 offset:6656\n"
        "ds_read_b64_tr_b16 a[88:89], v8 offset:6144\n"
        "ds_read_b64_tr_b16 a[90:91], v8 offset:6656\n"
        "buffer_load_dwordx4 v22, s[16:19], s60 offen lds\n"
        "s_bfe_u32 s61, s63, 0x40018\n"
        "s_add_u32 m0, s83, 0x0\n"
        "s_mul_i32 s60, s61, 0x2a000\n"
        "buffer_load_dwordx4 v22, s[16:19], s60 offen lds\n"
        "s_waitcnt lgkmcnt(6)\n"
        "v_mfma_f32_32x32x16_bf16 v[96:111], a[64:67], v[128:131], 0\n"
        "ds_read_b64_tr_b16 a[92:93], v9 offset:6144\n"
        "ds_read_b64_tr_b16 a[94:95], v9 offset:6656\n"
        "s_waitcnt lgkmcnt(15)\n"
        "v_mfma_f32_16x16x32_bf16 a[100:103], a[96:99], a[64:67], 0\n"
        "v_mfma_f32_32x32x16_bf16 v[96:111], a[68:71], v[132:135], v[96:111]\n"
        "ds_read_b64_tr_b16 a[64:65], v6 offset:8192\n"
        "ds_read_b64_tr_b16 a[66:67], v6 offset:8704\n"
        "v_mul_f32 v48, v32, v48\n"
        "v_mul_f32 v49, v33, v49\n"
        "v_cvt_pk_bf16_f32 v80, v48, v49\n"
        "v_mul_f32 v50, v34, v50\n"
        "v_mul_f32 v51, v35, v51\n"
        "v_cvt_pk_bf16_f32 v81, v50, v51\n"
        "v_mfma_f32_32x32x16_bf16 v[96:111], a[72:75], v[136:139], v[96:111]\n"
        "ds_read_b64_tr_b16 a[68:69], v7 offset:8192\n"
        "ds_read_b64_tr_b16 a[70:71], v7 offset:8704\n"
        "v_mul_f32 v52, v36, v52\n"
        "v_mul_f32 v53, v37, v53\n"
        "v_cvt_pk_bf16_f32 v82, v52, v53\n"
        "v_mul_f32 v54, v38, v54\n"
        "v_mul_f32 v55, v39, v55\n"
        "v_cvt_pk_bf16_f32 v83, v54, v55\n"
        "v_mfma_f32_32x32x16_bf16 v[96:111], a[76:79], v[140:143], v[96:111]\n"
        "ds_read_b64_tr_b16 a[72:73], v8 offset:8192\n"
        "ds_read_b64_tr_b16 a[74:75], v8 offset:8704\n"
        "buffer_store_dwordx4 v[80:83], v248, s[32:35], 0 offen offset:0 sc1\n"
        "v_mul_f32 v56, v40, v56\n"
        "v_mul_f32 v57, v41, v57\n"
        "v_cvt_pk_bf16_f32 v84, v56, v57\n"
        "v_mul_f32 v58, v42, v58\n"
        "s_waitcnt lgkmcnt(6)\n"
        "v_mfma_f32_32x32x16_bf16 v[96:111], a[80:83], v[144:147], v[96:111]\n"
        "ds_read_b64_tr_b16 a[76:77], v9 offset:8192\n"
        "ds_read_b64_tr_b16 a[78:79], v9 offset:8704\n"
        "v_mul_f32 v59, v43, v59\n"
        "v_cvt_pk_bf16_f32 v85, v58, v59\n"
        "v_mul_f32 v60, v44, v60\n"
        "v_mul_f32 v61, v45, v61\n"
        "v_cvt_pk_bf16_f32 v86, v60, v61\n"
        "v_mul_f32 v62, v46, v62\n"
        "v_mfma_f32_16x16x32_bf16 a[100:103], a[96:99], a[80:83], a[100:103]\n"
        "v_mul_f32 v63, v47, v63\n"
        "v_cvt_pk_bf16_f32 v87, v62, v63\n"
        "buffer_store_dwordx4 v[84:87], v248, s[32:35], 0 offen offset:1024 sc1\n"
        "v_mul_f32 v64, v32, v64\n"
        "v_mul_f32 v65, v33, v65\n"
        "v_mfma_f32_32x32x16_bf16 v[96:111], a[84:87], v[148:151], v[96:111]\n"
        "s_waitcnt vmcnt(9)\n"
        "s_barrier\n"
        "ds_read_b64_tr_b16 a[80:81], v6 offset:10240\n"
        "ds_read_b64_tr_b16 a[82:83], v6 offset:10752\n"
        "v_cvt_pk_bf16_f32 v80, v64, v65\n"
        "v_mul_f32 v66, v34, v66\n"
        "v_mfma_f32_32x32x16_bf16 v[96:111], a[88:91], v[152:155], v[96:111]\n"
        "ds_read_b64_tr_b16 a[84:85], v7 offset:10240\n"
        "ds_read_b64_tr_b16 a[86:87], v7 offset:10752\n"
        "v_mul_f32 v67, v35, v67\n"
        "v_cvt_pk_bf16_f32 v81, v66, v67\n"
        "v_mul_f32 v68, v36, v68\n"
        "v_mul_f32 v69, v37, v69\n"
        "v_cvt_pk_bf16_f32 v82, v68, v69\n"
        "v_mul_f32 v70, v38, v70\n"
        "v_mfma_f32_32x32x16_bf16 v[96:111], a[92:95], v[156:159], v[96:111]\n"
        "ds_read_b64_tr_b16 a[88:89], v8 offset:10240\n"
        "ds_read_b64_tr_b16 a[90:91], v8 offset:10752\n"
        "v_mul_f32 v71, v39, v71\n"
        "v_cvt_pk_bf16_f32 v83, v70, v71\n"
        "buffer_store_dwordx4 v[80:83], v249, s[32:35], 0 offen offset:0 sc1\n"
        "v_mul_f32 v72, v40, v72\n"
        "v_mul_f32 v73, v41, v73\n"
        "s_waitcnt lgkmcnt(6)\n"
        "v_mfma_f32_32x32x16_bf16 v[96:111], a[64:67], v[160:163], v[96:111]\n"
        "ds_read_b64_tr_b16 a[92:93], v9 offset:10240\n"
        "ds_read_b64_tr_b16 a[94:95], v9 offset:10752\n"
        "v_cvt_pk_bf16_f32 v84, v72, v73\n"
        "v_mul_f32 v74, v42, v74\n"
        "v_mul_f32 v75, v43, v75\n"
        "v_cvt_pk_bf16_f32 v85, v74, v75\n"
        "v_mul_f32 v76, v44, v76\n"
        "v_mul_f32 v77, v45, v77\n"
        "v_mfma_f32_16x16x32_bf16 a[100:103], a[96:99], a[64:67], a[100:103]\n"
        "v_cvt_pk_bf16_f32 v86, v76, v77\n"
        "v_mul_f32 v78, v46, v78\n"
        "v_mul_f32 v79, v47, v79\n"
        "v_cvt_pk_bf16_f32 v87, v78, v79\n"
        "buffer_store_dwordx4 v[84:87], v249, s[32:35], 0 offen offset:1024 sc1\n"
        "v_mfma_f32_32x32x16_bf16 v[96:111], a[68:71], v[164:167], v[96:111]\n"
        "ds_read_b64_tr_b16 a[64:65], v6 offset:12288\n"
        "ds_read_b64_tr_b16 a[66:67], v6 offset:12800\n"
        "v_mfma_f32_32x32x16_bf16 v[96:111], a[72:75], v[168:171], v[96:111]\n"
        "ds_read_b64_tr_b16 a[68:69], v7 offset:12288\n"
        "ds_read_b64_tr_b16 a[70:71], v7 offset:12800\n"
        "v_mfma_f32_32x32x16_bf16 v[96:111], a[76:79], v[172:175], v[96:111]\n"
        "ds_read_b64_tr_b16 a[72:73], v8 offset:12288\n"
        "ds_read_b64_tr_b16 a[74:75], v8 offset:12800\n"
        "role_join_%=:\n"
        "s_waitcnt lgkmcnt(6)\n"
        "v_mfma_f32_32x32x16_bf16 v[96:111], a[80:83], v[176:179], v[96:111]\n"
        "ds_read_b64_tr_b16 a[76:77], v9 offset:12288\n"
        "ds_read_b64_tr_b16 a[78:79], v9 offset:12800\n"
        "s_bfe_u32 s61, s63, 0x40000\n"
        "s_add_u32 m0, s83, 0x6000\n"
        "s_mul_i32 s60, s61, 0x38000\n"
        "v_mfma_f32_16x16x32_bf16 a[100:103], a[96:99], a[80:83], a[100:103]\n"
        "buffer_load_dwordx4 v20, s[24:27], s60 offen lds\n"
        "s_lshl_b32 s68, s81, 6\n"
        "v_mfma_f32_32x32x16_bf16 v[96:111], a[84:87], v[180:183], v[96:111]\n"
        "ds_read_b64_tr_b16 a[80:81], v6 offset:14336\n"
        "ds_read_b64_tr_b16 a[82:83], v6 offset:14848\n"
        "s_add_u32 s68, s68, s84\n"
        "s_mul_i32 s68, s68, 32\n"
        "s_add_u32 s68, s68, 0x1000\n"
        "v_mfma_f32_32x32x16_bf16 v[96:111], a[88:91], v[184:187], v[96:111]\n"
        "ds_read_b64_tr_b16 a[84:85], v7 offset:14336\n"
        "ds_read_b64_tr_b16 a[86:87], v7 offset:14848\n"
        "s_mul_i32 s64, s84, 114688\n"
        "s_lshl_b32 s97, s82, 12\n"
        "s_add_u32 s64, s64, s97\n"
        "v_mfma_f32_32x32x16_bf16 v[96:111], a[92:95], v[188:191], v[96:111]\n"
        "ds_read_b64_tr_b16 a[88:89], v8 offset:14336\n"
        "ds_read_b64_tr_b16 a[90:91], v8 offset:14848\n"
        "buffer_load_dwordx4 v21, s[24:27], s60 offen offset:1024 lds\n"
        "s_bfe_u32 s61, s63, 0x40004\n"
        "s_waitcnt lgkmcnt(6)\n"
        "v_mfma_f32_32x32x16_bf16 v[96:111], a[64:67], v[192:195], v[96:111]\n"
        "ds_read_b64_tr_b16 a[92:93], v9 offset:14336\n"
        "ds_read_b64_tr_b16 a[94:95], v9 offset:14848\n"
        "s_add_u32 m0, s83, 0x6800\n"
        "s_mul_i32 s60, s61, 0x38000\n"
        "s_add_u32 s64, s64, 0x500000\n"
        "v_mfma_f32_16x16x32_bf16 a[100:103], a[96:99], a[64:67], a[100:103]\n"
        "s_add_u32 s65, s64, 4096\n"
        "v_lshrrev_b32 v30, 9, v1\n"
        "v_lshlrev_b32 v30, 9, v30\n"
        "v_mfma_f32_32x32x16_bf16 v[96:111], a[68:71], v[196:199], v[96:111]\n"
        "ds_read_b64_tr_b16 a[64:65], v6 offset:16384\n"
        "ds_read_b64_tr_b16 a[66:67], v6 offset:16896\n"
        "buffer_load_dwordx4 v20, s[24:27], s60 offen lds\n"
        "v_bfe_u32 v31, v1, 6, 2\n"
        "v_mfma_f32_32x32x16_bf16 v[96:111], a[72:75], v[200:203], v[96:111]\n"
        "ds_read_b64_tr_b16 a[68:69], v7 offset:16384\n"
        "ds_read_b64_tr_b16 a[70:71], v7 offset:16896\n"
        "v_lshl_add_u32 v30, v31, 6, v30\n"
        "v_bfe_u32 v31, v1, 8, 1\n"
        "v_lshl_add_u32 v30, v31, 5, v30\n"
        "v_mfma_f32_32x32x16_bf16 v[96:111], a[76:79], v[204:207], v[96:111]\n"
        "ds_read_b64_tr_b16 a[72:73], v8 offset:16384\n"
        "ds_read_b64_tr_b16 a[74:75], v8 offset:16896\n"
        "v_bfe_u32 v31, v1, 4, 2\n"
        "v_lshl_add_u32 v30, v31, 3, v30\n"
        "v_add_u32 v2, s83, v30\n"
        "s_waitcnt lgkmcnt(6)\n"
        "v_mfma_f32_32x32x16_bf16 v[96:111], a[80:83], v[208:211], v[96:111]\n"
        "ds_read_b64_tr_b16 a[76:77], v9 offset:16384\n"
        "ds_read_b64_tr_b16 a[78:79], v9 offset:16896\n"
        "buffer_load_dwordx4 v21, s[24:27], s60 offen offset:1024 lds\n"
        "s_bfe_u32 s61, s63, 0x40008\n"
        "v_mfma_f32_16x16x32_bf16 a[100:103], a[96:99], a[80:83], a[100:103]\n"
        "s_add_u32 m0, s83, 0x7000\n"
        "s_mul_i32 s60, s61, 0x38000\n"
        "s_add_u32 s96, s78, 1\n"
        "v_mfma_f32_32x32x16_bf16 v[96:111], a[84:87], v[212:215], v[96:111]\n"
        "ds_read_b64_tr_b16 a[80:81], v10 offset:4096\n"
        "ds_read_b64_tr_b16 a[82:83], v10 offset:4608\n"
        "s_and_b32 s96, s96, 3\n"
        "s_mul_i32 s96, s96, 40960\n"
        "s_add_u32 s94, s96, %3\n"
        "v_mfma_f32_32x32x16_bf16 v[96:111], a[88:91], v[216:219], v[96:111]\n"
        "ds_read_b64_tr_b16 a[84:85], v10 offset:6144\n"
        "ds_read_b64_tr_b16 a[86:87], v10 offset:6656\n"
        "buffer_load_dwordx4 v20, s[24:27], s60 offen lds\n"
        "v_add_u32 v3, s94, v30\n"
        "v_mfma_f32_32x32x16_bf16 v[96:111], a[92:95], v[220:223], v[96:111]\n"
        "ds_read_b64_tr_b16 a[88:89], v10 offset:8192\n"
        "ds_read_b64_tr_b16 a[90:91], v10 offset:8704\n"
        "s_add_u32 s96, s78, 2\n"
        "s_and_b32 s96, s96, 3\n"
        "s_mul_i32 s96, s96, 40960\n"
        "s_waitcnt lgkmcnt(6)\n"
        "v_mfma_f32_32x32x16_bf16 v[96:111], a[64:67], v[224:227], v[96:111]\n"
        "ds_read_b64_tr_b16 a[92:93], v10 offset:10240\n"
        "ds_read_b64_tr_b16 a[94:95], v10 offset:10752\n"
        "s_add_u32 s76, s96, %3\n"
        "buffer_load_dwordx4 v21, s[24:27], s60 offen offset:1024 lds\n"
        "v_mfma_f32_16x16x32_bf16 a[100:103], a[96:99], a[64:67], a[100:103]\n"
        "s_bfe_u32 s61, s63, 0x4000c\n"
        "s_add_u32 m0, s83, 0x7800\n"
        "s_mul_i32 s60, s61, 0x38000\n"
        "v_mfma_f32_32x32x16_bf16 v[96:111], a[68:71], v[228:231], v[96:111]\n"
        "ds_read_b64_tr_b16 a[64:65], v10 offset:12288\n"
        "ds_read_b64_tr_b16 a[66:67], v10 offset:12800\n"
        "v_add_u32 v4, s76, v30\n"
        "s_add_u32 s96, s78, 3\n"
        "s_and_b32 s96, s96, 3\n"
        "v_mfma_f32_32x32x16_bf16 v[96:111], a[72:75], v[232:235], v[96:111]\n"
        "ds_read_b64_tr_b16 a[68:69], v10 offset:14336\n"
        "ds_read_b64_tr_b16 a[70:71], v10 offset:14848\n"
        "buffer_load_dwordx4 v20, s[24:27], s60 offen lds\n"
        "ds_write_b32 v18, a100 offset:0\n"
        "v_mfma_f32_32x32x16_bf16 v[96:111], a[76:79], v[236:239], v[96:111]\n"
        "ds_read_b64_tr_b16 a[72:73], v10 offset:16384\n"
        "ds_read_b64_tr_b16 a[74:75], v10 offset:16896\n"
        "s_mul_i32 s96, s96, 40960\n"
        "s_add_u32 s77, s96, %3\n"
        "v_add_u32 v5, s77, v30\n"
        "s_waitcnt lgkmcnt(7)\n"
        "v_mfma_f32_32x32x16_bf16 v[48:63], a[80:83], v[128:131], 0\n"
        "ds_read_b64_tr_b16 a[76:77], v11 offset:12288\n"
        "ds_read_b64_tr_b16 a[78:79], v11 offset:12800\n"
        "buffer_load_dwordx4 v21, s[24:27], s60 offen offset:1024 lds\n"
        "s_bfe_u32 s61, s63, 0x40010\n"
        "v_mfma_f32_16x16x32_bf16 a[104:107], a[96:99], a[80:83], 0\n"
        "s_add_u32 m0, s83, 0x8000\n"
        "s_mul_i32 s60, s61, 0x38000\n"
        "s_mul_i32 s97, s78, 1048576\n"
        "v_mfma_f32_32x32x16_bf16 v[48:63], a[84:87], v[144:147], v[48:63]\n"
        "ds_read_b64_tr_b16 a[80:81], v12 offset:12288\n"
        "ds_read_b64_tr_b16 a[82:83], v12 offset:12800\n"
        "s_waitcnt lgkmcnt(6)\n"
        "s_barrier\n"
        "v_mfma_f32_16x16x32_bf16 a[104:107], a[96:99], a[84:87], a[104:107]\n"
        "ds_read_b128 v[120:123], v19 offset:0\n"
        "v_accvgpr_read_b32 v117, a108\n"
        "v_mfma_f32_32x32x16_bf16 v[48:63], a[88:91], v[160:163], v[48:63]\n"
        "ds_read_b64_tr_b16 a[84:85], v13 offset:12288\n"
        "ds_read_b64_tr_b16 a[86:87], v13 offset:12800\n"
        "buffer_load_dwordx4 v20, s[24:27], s60 offen lds\n"
        "s_add_u32 s97, s97, 0x100000\n"
        "v_mfma_f32_16x16x32_bf16 a[104:107], a[96:99], a[88:91], a[104:107]\n"
        "s_add_u32 s95, s28, s97\n"
        "s_addc_u32 s13, s29, 0\n"
        "s_mov_b32 s14, 0x10000\n"
        "v_mfma_f32_32x32x16_bf16 v[48:63], a[92:95], v[176:179], v[48:63]\n"
        "ds_read_b64_tr_b16 a[88:89], v11 offset:14336\n"
        "ds_read_b64_tr_b16 a[90:91], v11 offset:14848\n"
        "s_waitcnt lgkmcnt(4)\n"
        "v_add_f32 v120, v120, v121\n"
        "v_add_f32 v122, v122, v123\n"
        "v_add_f32 v120, v120, v122\n"
        "v_mfma_f32_16x16x32_bf16 a[104:107], a[96:99], a[92:95], a[104:107]\n"
        "v_cvt_pk_bf16_f32 v121, v120, 0\n"
        "buffer_load_dwordx4 v21, s[24:27], s60 offen offset:1024 lds\n"
        "v_lshlrev_b32 v121, 16, v121\n"
        "s_waitcnt lgkmcnt(15)\n"
        "v_mfma_f32_32x32x16_bf16 v[48:63], a[64:67], v[192:195], v[48:63]\n"
        "ds_read_b64_tr_b16 a[92:93], v12 offset:14336\n"
        "ds_read_b64_tr_b16 a[94:95], v12 offset:14848\n"
        "s_bfe_u32 s61, s63, 0x40014\n"
        "s_add_u32 m0, s83, 0x8800\n"
        "s_mul_i32 s60, s61, 0x38000\n"
        "v_sub_f32 v121, v120, v121\n"
        "v_mfma_f32_16x16x32_bf16 a[104:107], a[96:99], a[64:67], a[104:107]\n"
        "v_cvt_pk_bf16_f32 v116, v120, v121\n"
        "s_and_b32 s62, s79, 7\n"
        "s_mul_i32 s62, s62, 0\n"
        "v_mfma_f32_32x32x16_bf16 v[96:111], v[116:119], v[112:115], v[96:111]\n"
        "buffer_load_dwordx4 v20, s[24:27], s60 offen lds\n"
        "s_or_b32 s12, s95, s62\n"
        "s_mul_i32 s96, s80, 7\n"
        "s_lshr_b32 s97, s96, 1\n"
        "s_lshl_b32 s97, s97, 9\n"
        "v_add_u32 v28, s97, v1\n"
        "v_mfma_f32_32x32x16_bf16 v[48:63], a[68:71], v[208:211], v[48:63]\n"
        "ds_read_b64_tr_b16 a[64:65], v13 offset:14336\n"
        "ds_read_b64_tr_b16 a[66:67], v13 offset:14848\n"
        "s_lshl_b32 s97, s80, 11\n"
        "s_add_u32 s97, s97, 28672\n"
        "v_add_u32 v29, s97, v1\n"
        "v_mfma_f32_16x16x32_bf16 a[104:107], a[96:99], a[68:71], a[104:107]\n"
        "s_add_u32 s97, s96, 6\n"
        "s_lshr_b32 s96, s96, 4\n"
        "s_lshr_b32 s97, s97, 4\n"
        "v_mfma_f32_32x32x16_bf16 v[48:63], a[72:75], v[224:227], v[48:63]\n"
        "ds_read_b64_tr_b16 a[68:69], v11 offset:4096\n"
        "ds_read_b64_tr_b16 a[70:71], v11 offset:4608\n"
        "buffer_load_dwordx4 v21, s[24:27], s60 offen offset:1024 lds\n"
        "s_bfe_u32 s61, s63, 0x40018\n"
        "v_max_f32 v96, 0, v96\n"
        "v_mfma_f32_16x16x32_bf16 a[104:107], a[96:99], a[72:75], a[104:107]\n"
        "s_add_u32 m0, s83, 0x9000\n"
        "s_mul_i32 s60, s61, 0x38000\n"
        "v_max_f32 v97, 0, v97\n"
        "v_mul_f32 v96, v26, v96\n"
        "v_mfma_f32_32x32x16_bf16 v[48:63], a[76:79], v[196:199], v[48:63]\n"
        "ds_read_b64_tr_b16 a[72:73], v12 offset:4096\n"
        "ds_read_b64_tr_b16 a[74:75], v12 offset:4608\n"
        "v_mul_f32 v97, v26, v97\n"
        "v_mul_f32 v96, v96, v96\n"
        "v_mul_f32 v97, v97, v97\n"
        "v_cvt_pk_bf16_f32 v80, v96, v97\n"
        "s_waitcnt lgkmcnt(6)\n"
        "v_mfma_f32_32x32x16_bf16 v[48:63], a[80:83], v[200:203], v[48:63]\n"
        "ds_read_b64_tr_b16 a[76:77], v13 offset:4096\n"
        "ds_read_b64_tr_b16 a[78:79], v13 offset:4608\n"
        "s_waitcnt vmcnt(12)\n"
        "buffer_store_dwordx2 v[24:25], v23, s[32:35], s68 offen sc1\n"
        "v_max_f32 v98, 0, v98\n"
        "v_mfma_f32_32x32x16_bf16 v[48:63], a[84:87], v[204:207], v[48:63]\n"
        "ds_read_b64_tr_b16 a[80:81], v11 offset:6144\n"
        "ds_read_b64_tr_b16 a[82:83], v11 offset:6656\n"
        "v_max_f32 v99, 0, v99\n"
        "v_mul_f32 v98, v26, v98\n"
        "v_mul_f32 v99, v26, v99\n"
        "v_mul_f32 v98, v98, v98\n"
        "v_mfma_f32_32x32x16_bf16 v[48:63], a[88:91], v[212:215], v[48:63]\n"
        "ds_read_b64_tr_b16 a[84:85], v12 offset:6144\n"
        "ds_read_b64_tr_b16 a[86:87], v12 offset:6656\n"
        "v_mul_f32 v99, v99, v99\n"
        "buffer_load_dwordx4 v20, s[24:27], s60 offen lds\n"
        "v_cvt_pk_bf16_f32 v81, v98, v99\n"
        "v_mfma_f32_32x32x16_bf16 v[48:63], a[92:95], v[216:219], v[48:63]\n"
        "ds_read_b64_tr_b16 a[88:89], v13 offset:6144\n"
        "ds_read_b64_tr_b16 a[90:91], v13 offset:6656\n"
        "v_max_f32 v100, 0, v100\n"
        "v_max_f32 v101, 0, v101\n"
        "v_mul_f32 v100, v26, v100\n"
        "v_mul_f32 v101, v26, v101\n"
        "s_waitcnt lgkmcnt(6)\n"
        "v_mfma_f32_32x32x16_bf16 v[48:63], a[64:67], v[220:223], v[48:63]\n"
        "ds_read_b64_tr_b16 a[92:93], v11 offset:8192\n"
        "ds_read_b64_tr_b16 a[94:95], v11 offset:8704\n"
        "buffer_load_dwordx4 v21, s[24:27], s60 offen offset:1024 lds\n"
        "v_mul_f32 v100, v100, v100\n"
        "v_mul_f32 v101, v101, v101\n"
        "v_mfma_f32_32x32x16_bf16 v[48:63], a[68:71], v[132:135], v[48:63]\n"
        "ds_read_b64_tr_b16 a[64:65], v12 offset:8192\n"
        "ds_read_b64_tr_b16 a[66:67], v12 offset:8704\n"
        "v_cvt_pk_bf16_f32 v82, v100, v101\n"
        "v_max_f32 v102, 0, v102\n"
        "v_max_f32 v103, 0, v103\n"
        "v_mul_f32 v102, v26, v102\n"
        "v_mfma_f32_32x32x16_bf16 v[48:63], a[72:75], v[136:139], v[48:63]\n"
        "ds_read_b64_tr_b16 a[68:69], v13 offset:8192\n"
        "ds_read_b64_tr_b16 a[70:71], v13 offset:8704\n"
        "v_mul_f32 v103, v26, v103\n"
        "v_mul_f32 v102, v102, v102\n"
        "v_mul_f32 v103, v103, v103\n"
        "v_cvt_pk_bf16_f32 v83, v102, v103\n"
        "v_mfma_f32_32x32x16_bf16 v[48:63], a[76:79], v[140:143], v[48:63]\n"
        "ds_read_b64_tr_b16 a[72:73], v11 offset:10240\n"
        "ds_read_b64_tr_b16 a[74:75], v11 offset:10752\n"
        "buffer_store_dwordx4 v[80:83], v1, s[28:31], s64 offen offset:0 sc1\n"
        "v_max_f32 v104, 0, v104\n"
        "v_max_f32 v105, 0, v105\n"
        "s_waitcnt lgkmcnt(6)\n"
        "v_mfma_f32_32x32x16_bf16 v[48:63], a[80:83], v[148:151], v[48:63]\n"
        "ds_read_b64_tr_b16 a[76:77], v12 offset:10240\n"
        "ds_read_b64_tr_b16 a[78:79], v12 offset:10752\n"
        "v_mul_f32 v104, v26, v104\n"
        "v_mul_f32 v105, v26, v105\n"
        "v_mul_f32 v104, v104, v104\n"
        "v_mul_f32 v105, v105, v105\n"
        "v_mfma_f32_32x32x16_bf16 v[48:63], a[84:87], v[152:155], v[48:63]\n"
        "ds_read_b64_tr_b16 a[80:81], v13 offset:10240\n"
        "ds_read_b64_tr_b16 a[82:83], v13 offset:10752\n"
        "v_cvt_pk_bf16_f32 v84, v104, v105\n"
        "v_max_f32 v106, 0, v106\n"
        "v_max_f32 v107, 0, v107\n"
        "v_mul_f32 v106, v26, v106\n"
        "v_mfma_f32_32x32x16_bf16 v[48:63], a[88:91], v[156:159], v[48:63]\n"
        "ds_read_b64_tr_b16 a[84:85], v11 offset:16384\n"
        "ds_read_b64_tr_b16 a[86:87], v11 offset:16896\n"
        "v_mul_f32 v107, v26, v107\n"
        "v_mul_f32 v106, v106, v106\n"
        "v_mul_f32 v107, v107, v107\n"
        "v_cvt_pk_bf16_f32 v85, v106, v107\n"
        "v_mfma_f32_32x32x16_bf16 v[48:63], a[92:95], v[164:167], v[48:63]\n"
        "ds_read_b64_tr_b16 a[88:89], v12 offset:16384\n"
        "ds_read_b64_tr_b16 a[90:91], v12 offset:16896\n"
        "v_max_f32 v108, 0, v108\n"
        "v_max_f32 v109, 0, v109\n"
        "v_mul_f32 v108, v26, v108\n"
        "v_mul_f32 v109, v26, v109\n"
        "s_waitcnt lgkmcnt(6)\n"
        "v_mfma_f32_32x32x16_bf16 v[48:63], a[64:67], v[168:171], v[48:63]\n"
        "ds_read_b64_tr_b16 a[92:93], v13 offset:16384\n"
        "ds_read_b64_tr_b16 a[94:95], v13 offset:16896\n"
        "v_mul_f32 v108, v108, v108\n"
        "v_mul_f32 v109, v109, v109\n"
        "v_cvt_pk_bf16_f32 v86, v108, v109\n"
        "v_max_f32 v110, 0, v110\n"
        "v_mfma_f32_32x32x16_bf16 v[48:63], a[68:71], v[172:175], v[48:63]\n"
        "s_waitcnt vmcnt(63)\n"
        "s_barrier\n"
        "ds_read_b64_tr_b16 a[64:65], v2 offset:18432\n"
        "ds_read_b64_tr_b16 a[66:67], v2 offset:18688\n"
        "v_mfma_f32_32x32x16_bf16 v[48:63], a[72:75], v[180:183], v[48:63]\n"
        "ds_read_b64_tr_b16 a[68:69], v3 offset:18432\n"
        "ds_read_b64_tr_b16 a[70:71], v3 offset:18688\n"
        "v_max_f32 v111, 0, v111\n"
        "v_mul_f32 v110, v26, v110\n"
        "v_mul_f32 v111, v26, v111\n"
        "v_mul_f32 v110, v110, v110\n"
        "v_mfma_f32_32x32x16_bf16 v[48:63], a[76:79], v[184:187], v[48:63]\n"
        "ds_read_b64_tr_b16 a[72:73], v4 offset:18432\n"
        "ds_read_b64_tr_b16 a[74:75], v4 offset:18688\n"
        "v_mul_f32 v111, v111, v111\n"
        "v_cvt_pk_bf16_f32 v87, v110, v111\n"
        "buffer_store_dwordx4 v[84:87], v1, s[28:31], s64 offen offset:1024 sc1\n"
        "s_waitcnt lgkmcnt(6)\n"
        "v_mfma_f32_32x32x16_bf16 v[48:63], a[80:83], v[188:191], v[48:63]\n"
        "ds_read_b64_tr_b16 a[76:77], v5 offset:18432\n"
        "ds_read_b64_tr_b16 a[78:79], v5 offset:18688\n"
        "ds_write_b32 v18, a104 offset:40960\n"
        "s_sub_u32 s97, s97, s96\n"
        "v_mfma_f32_32x32x16_bf16 v[48:63], a[84:87], v[228:231], v[48:63]\n"
        "ds_read_b64_tr_b16 a[80:81], v2 offset:19456\n"
        "ds_read_b64_tr_b16 a[82:83], v2 offset:19712\n"
        "v_bfe_u32 v31, v1, 8, 1\n"
        "v_mul_u32_u24 v31, s97, v31\n"
        "v_lshrrev_b32 v30, 9, v1\n"
        "v_mfma_f32_32x32x16_bf16 v[48:63], a[88:91], v[232:235], v[48:63]\n"
        "ds_read_b64_tr_b16 a[84:85], v3 offset:19456\n"
        "ds_read_b64_tr_b16 a[86:87], v3 offset:19712\n"
        "s_waitcnt lgkmcnt(4)\n"
        "s_barrier\n"
        "v_mfma_f32_32x32x16_bf16 v[48:63], a[92:95], v[236:239], v[48:63]\n"
        "ds_read_b64_tr_b16 a[88:89], v4 offset:19456\n"
        "ds_read_b64_tr_b16 a[90:91], v4 offset:19712\n"
        "ds_read_b128 v[120:123], v19 offset:40960\n"
        "v_accvgpr_read_b32 v117, a109\n"
        "s_waitcnt lgkmcnt(15)\n"
        "v_mfma_f32_32x32x16_bf16 v[64:79], a[64:67], v[128:131], 0\n"
        "ds_read_b64_tr_b16 a[92:93], v5 offset:19456\n"
        "ds_read_b64_tr_b16 a[94:95], v5 offset:19712\n"
        "v_mad_u32_u24 v31, v30, 7, v31\n"
        "s_lshl_b32 s96, s96, 6\n"
        "s_lshl_b32 s97, s78, 4\n"
        "v_mfma_f32_16x16x32_bf16 a[100:103], a[96:99], a[64:67], 0\n"
        "s_add_u32 s96, s96, s97\n"
        "v_lshlrev_b32 v31, 6, v31\n"
        "v_bfe_u32 v30, v1, 4, 4\n"
        "v_mfma_f32_32x32x16_bf16 v[64:79], a[68:71], v[132:135], v[64:79]\n"
        "ds_read_b64_tr_b16 a[64:65], v2 offset:20480\n"
        "ds_read_b64_tr_b16 a[66:67], v2 offset:20736\n"
        "s_waitcnt lgkmcnt(4)\n"
        "v_add_f32 v120, v120, v121\n"
        "v_add_f32 v122, v122, v123\n"
        "v_add_f32 v120, v120, v122\n"
        "v_mfma_f32_32x32x16_bf16 v[64:79], a[72:75], v[136:139], v[64:79]\n"
        "ds_read_b64_tr_b16 a[68:69], v3 offset:20480\n"
        "ds_read_b64_tr_b16 a[70:71], v3 offset:20736\n"
        "v_cvt_pk_bf16_f32 v121, v120, 0\n"
        "v_lshlrev_b32 v121, 16, v121\n"
        "v_sub_f32 v121, v120, v121\n"
        "v_cvt_pk_bf16_f32 v116, v120, v121\n"
        "v_mfma_f32_32x32x16_bf16 v[64:79], a[76:79], v[140:143], v[64:79]\n"
        "ds_read_b64_tr_b16 a[72:73], v4 offset:20480\n"
        "ds_read_b64_tr_b16 a[74:75], v4 offset:20736\n"
        "v_add3_u32 v30, v30, v31, s96\n"
        "v_mul_u32_u24 v30, 32, v30\n"
        "v_add_u32 v22, 0x1000, v30\n"
        "v_mfma_f32_32x32x16_bf16 v[48:63], v[116:119], v[112:115], v[48:63]\n"
        "s_bitcmp1_b32 s80, 0\n"
        "s_cselect_b32 s6, -1, 0\n"
        "s_cselect_b32 s5, 0, -1\n"
        "s_mov_b32 s7, 0\n"
        "s_mov_b32 s4, 0\n"
        "v_mov_b32 v252, s91\n"
        "v_mov_b32 v253, 0x3f80\n"
        "s_waitcnt lgkmcnt(6)\n"
        "v_mfma_f32_32x32x16_bf16 v[64:79], a[80:83], v[144:147], v[64:79]\n"
        "ds_read_b64_tr_b16 a[76:77], v5 offset:20480\n"
        "ds_read_b64_tr_b16 a[78:79], v5 offset:20736\n"
        "s_nop 0\n"
        "v_max_f32 v48, 0, v48\n"
        "v_max_f32 v49, 0, v49\n"
        "v_mul_f32 v48, v26, v48\n"
        "v_mul_f32 v49, v26, v49\n"
        "v_mfma_f32_16x16x32_bf16 a[100:103], a[96:99], a[80:83], a[100:103]\n"
        "v_mul_f32 v48, v48, v48\n"
        "v_mul_f32 v49, v49, v49\n"
        "v_cvt_pk_bf16_f32 v80, v48, v49\n"
        "v_max_f32 v50, 0, v50\n"
        "v_mfma_f32_32x32x16_bf16 v[64:79], a[84:87], v[148:151], v[64:79]\n"
        "ds_read_b64_tr_b16 a[80:81], v2 offset:21504\n"
        "ds_read_b64_tr_b16 a[82:83], v2 offset:21760\n"
        "v_max_f32 v51, 0, v51\n"
        "v_mul_f32 v50, v26, v50\n"
        "v_mul_f32 v51, v26, v51\n"
        "v_mul_f32 v50, v50, v50\n"
        "v_mfma_f32_32x32x16_bf16 v[64:79], a[88:91], v[152:155], v[64:79]\n"
        "ds_read_b64_tr_b16 a[84:85], v3 offset:21504\n"
        "ds_read_b64_tr_b16 a[86:87], v3 offset:21760\n"
        "v_mul_f32 v51, v51, v51\n"
        "v_cvt_pk_bf16_f32 v81, v50, v51\n"
        "v_max_f32 v52, 0, v52\n"
        "v_max_f32 v53, 0, v53\n"
        "v_mfma_f32_32x32x16_bf16 v[64:79], a[92:95], v[156:159], v[64:79]\n"
        "ds_read_b64_tr_b16 a[88:89], v4 offset:21504\n"
        "ds_read_b64_tr_b16 a[90:91], v4 offset:21760\n"
        "v_mul_f32 v52, v26, v52\n"
        "v_mul_f32 v53, v26, v53\n"
        "v_mul_f32 v52, v52, v52\n"
        "v_mul_f32 v53, v53, v53\n"
        "s_waitcnt lgkmcnt(6)\n"
        "v_mfma_f32_32x32x16_bf16 v[64:79], a[64:67], v[160:163], v[64:79]\n"
        "ds_read_b64_tr_b16 a[92:93], v5 offset:21504\n"
        "ds_read_b64_tr_b16 a[94:95], v5 offset:21760\n"
        "v_cvt_pk_bf16_f32 v82, v52, v53\n"
        "v_max_f32 v54, 0, v54\n"
        "v_max_f32 v55, 0, v55\n"
        "v_mul_f32 v54, v26, v54\n"
        "v_mfma_f32_16x16x32_bf16 a[100:103], a[96:99], a[64:67], a[100:103]\n"
        "v_mul_f32 v55, v26, v55\n"
        "v_mul_f32 v54, v54, v54\n"
        "v_mul_f32 v55, v55, v55\n"
        "v_cvt_pk_bf16_f32 v83, v54, v55\n"
        "v_mfma_f32_32x32x16_bf16 v[64:79], a[68:71], v[164:167], v[64:79]\n"
        "s_waitcnt vmcnt(63)\n"
        "s_barrier\n"
        "ds_read_b64_tr_b16 a[64:65], v2 offset:22528\n"
        "ds_read_b64_tr_b16 a[66:67], v2 offset:22784\n"
        "v_mfma_f32_32x32x16_bf16 v[64:79], a[72:75], v[168:171], v[64:79]\n"
        "ds_read_b64_tr_b16 a[68:69], v3 offset:22528\n"
        "ds_read_b64_tr_b16 a[70:71], v3 offset:22784\n"
        "buffer_store_dwordx4 v[80:83], v1, s[28:31], s64 offen offset:2048 sc1\n"
        "v_max_f32 v56, 0, v56\n"
        "v_max_f32 v57, 0, v57\n"
        "v_mfma_f32_32x32x16_bf16 v[64:79], a[76:79], v[172:175], v[64:79]\n"
        "ds_read_b64_tr_b16 a[72:73], v4 offset:22528\n"
        "ds_read_b64_tr_b16 a[74:75], v4 offset:22784\n"
        "v_mul_f32 v56, v26, v56\n"
        "v_mul_f32 v57, v26, v57\n"
        "v_mul_f32 v56, v56, v56\n"
        "v_mul_f32 v57, v57, v57\n"
        "s_waitcnt lgkmcnt(6)\n"
        "v_mfma_f32_32x32x16_bf16 v[64:79], a[80:83], v[176:179], v[64:79]\n"
        "ds_read_b64_tr_b16 a[76:77], v5 offset:22528\n"
        "ds_read_b64_tr_b16 a[78:79], v5 offset:22784\n"
        "v_cvt_pk_bf16_f32 v84, v56, v57\n"
        "v_max_f32 v58, 0, v58\n"
        "v_max_f32 v59, 0, v59\n"
        "v_mul_f32 v58, v26, v58\n"
        "v_mfma_f32_16x16x32_bf16 a[100:103], a[96:99], a[80:83], a[100:103]\n"
        "v_mul_f32 v59, v26, v59\n"
        "v_mul_f32 v58, v58, v58\n"
        "v_mul_f32 v59, v59, v59\n"
        "v_cvt_pk_bf16_f32 v85, v58, v59\n"
        "v_mfma_f32_32x32x16_bf16 v[64:79], a[84:87], v[180:183], v[64:79]\n"
        "ds_read_b64_tr_b16 a[80:81], v2 offset:23552\n"
        "ds_read_b64_tr_b16 a[82:83], v2 offset:23808\n"
        "v_max_f32 v60, 0, v60\n"
        "v_max_f32 v61, 0, v61\n"
        "v_mul_f32 v60, v26, v60\n"
        "v_mul_f32 v61, v26, v61\n"
        "v_mfma_f32_32x32x16_bf16 v[64:79], a[88:91], v[184:187], v[64:79]\n"
        "ds_read_b64_tr_b16 a[84:85], v3 offset:23552\n"
        "ds_read_b64_tr_b16 a[86:87], v3 offset:23808\n"
        "v_mul_f32 v60, v60, v60\n"
        "v_mul_f32 v61, v61, v61\n"
        "v_cvt_pk_bf16_f32 v86, v60, v61\n"
        "v_max_f32 v62, 0, v62\n"
        "v_mfma_f32_32x32x16_bf16 v[64:79], a[92:95], v[188:191], v[64:79]\n"
        "ds_read_b64_tr_b16 a[88:89], v4 offset:23552\n"
        "ds_read_b64_tr_b16 a[90:91], v4 offset:23808\n"
        "v_max_f32 v63, 0, v63\n"
        "v_mul_f32 v62, v26, v62\n"
        "v_mul_f32 v63, v26, v63\n"
        "v_mul_f32 v62, v62, v62\n"
        "s_waitcnt lgkmcnt(6)\n"
        "v_mfma_f32_32x32x16_bf16 v[64:79], a[64:67], v[192:195], v[64:79]\n"
        "ds_read_b64_tr_b16 a[92:93], v5 offset:23552\n"
        "ds_read_b64_tr_b16 a[94:95], v5 offset:23808\n"
        "buffer_load_dwordx2 v[250:251], v22, s[28:31], 0 offen sc1\n"
        "v_mul_f32 v63, v63, v63\n"
        "v_cvt_pk_bf16_f32 v87, v62, v63\n"
        "v_mfma_f32_16x16x32_bf16 a[100:103], a[96:99], a[64:67], a[100:103]\n"
        "buffer_store_dwordx4 v[84:87], v1, s[28:31], s64 offen offset:3072 sc1\n"
        "v_mov_b32 v254, 0\n"
        "v_mfma_f32_32x32x16_bf16 v[64:79], a[68:71], v[196:199], v[64:79]\n"
        "ds_read_b64_tr_b16 a[64:65], v2 offset:0\n"
        "ds_read_b64_tr_b16 a[66:67], v2 offset:256\n"
        "v_mov_b32 v255, 0\n"
        "v_cndmask_b32 v252, v252, 0, s[92:93]\n"
        "v_cndmask_b32 v253, v253, 0, s[92:93]\n"
        "v_mfma_f32_32x32x16_bf16 v[64:79], a[72:75], v[200:203], v[64:79]\n"
        "ds_read_b64_tr_b16 a[68:69], v3 offset:0\n"
        "ds_read_b64_tr_b16 a[70:71], v3 offset:256\n"
        "v_mov_b32 v126, 0\n"
        "v_mov_b32 v127, 0\n"
        "v_bfe_u32 v15, v1, 8, 1\n"
        "v_mfma_f32_32x32x16_bf16 v[64:79], a[76:79], v[204:207], v[64:79]\n"
        "ds_read_b64_tr_b16 a[72:73], v4 offset:0\n"
        "ds_read_b64_tr_b16 a[74:75], v4 offset:256\n"
        "v_lshlrev_b32 v15, 6, v15\n"
        "v_and_b32 v16, 0x70, v1\n"
        "v_xor_b32 v15, v15, v16\n"
        "s_waitcnt lgkmcnt(6)\n"
        "v_mfma_f32_32x32x16_bf16 v[64:79], a[80:83], v[208:211], v[64:79]\n"
        "ds_read_b64_tr_b16 a[76:77], v5 offset:0\n"
        "ds_read_b64_tr_b16 a[78:79], v5 offset:256\n"
        "v_lshrrev_b32 v16, 7, v1\n"
        "v_mul_u32_u24 v16, 896, v16\n"
        "v_add_u32 v14, v15, v16\n"
        "v_mfma_f32_16x16x32_bf16 a[100:103], a[96:99], a[80:83], a[100:103]\n"
        "v_add_u32 v15, 6144, v14\n"
        "v_add_u32 v16, 12288, v14\n"
        "v_add_u32 v17, 18432, v14\n"
        "v_mfma_f32_32x32x16_bf16 v[64:79], a[84:87], v[212:215], v[64:79]\n"
        "s_waitcnt vmcnt(5)\n"
        "s_barrier\n"
        "ds_read_b64_tr_b16 a[80:81], v6 offset:24576\n"
        "ds_read_b64_tr_b16 a[82:83], v6 offset:25088\n"
        "v_mfma_f32_32x32x16_bf16 v[64:79], a[88:91], v[216:219], v[64:79]\n"
        "ds_read_b64_tr_b16 a[84:85], v7 offset:24576\n"
        "ds_read_b64_tr_b16 a[86:87], v7 offset:25088\n"
        "v_lshrrev_b32 v249, 9, v1\n"
        "v_lshlrev_b32 v249, 3, v249\n"
        "v_bfe_u32 v248, v1, 4, 5\n"
        "v_mfma_f32_32x32x16_bf16 v[64:79], a[92:95], v[220:223], v[64:79]\n"
        "ds_read_b64_tr_b16 a[88:89], v8 offset:24576\n"
        "ds_read_b64_tr_b16 a[90:91], v8 offset:25088\n"
        "v_mul_u32_u24 v248, 896, v248\n"
        "v_add_u32 v248, v248, v249\n"
        "s_mul_i32 s98, s78, 401408\n"
        "s_waitcnt lgkmcnt(6)\n"
        "v_mfma_f32_32x32x16_bf16 v[64:79], a[64:67], v[224:227], v[64:79]\n"
        "ds_read_b64_tr_b16 a[92:93], v9 offset:24576\n"
        "ds_read_b64_tr_b16 a[94:95], v9 offset:25088\n"
        "s_lshl_b32 s67, s86, 7\n"
        "s_add_u32 s98, s98, s67\n"
        "v_mfma_f32_16x16x32_bf16 a[100:103], a[96:99], a[64:67], a[100:103]\n"
        "s_add_u32 s16, s44, s98\n"
        "s_addc_u32 s17, s45, 0\n"
        "s_and_b32 s98, s85, 3\n"
        "v_mfma_f32_32x32x16_bf16 v[64:79], a[68:71], v[228:231], v[64:79]\n"
        "ds_read_b64_tr_b16 a[64:65], v6 offset:26624\n"
        "ds_read_b64_tr_b16 a[66:67], v6 offset:27136\n"
        "s_lshl_b32 s98, s98, 1\n"
        "s_lshr_b32 s99, s85, 2\n"
        "s_add_u32 s98, s98, s99\n"
        "v_mfma_f32_32x32x16_bf16 v[64:79], a[72:75], v[232:235], v[64:79]\n"
        "ds_read_b64_tr_b16 a[68:69], v7 offset:26624\n"
        "ds_read_b64_tr_b16 a[70:71], v7 offset:27136\n"
        "ds_write_b32 v18, a100 offset:0\n"
        "s_add_u32 s99, s98, 0\n"
        "v_mfma_f32_32x32x16_bf16 v[64:79], a[76:79], v[236:239], v[64:79]\n"
        "ds_read_b64_tr_b16 a[72:73], v8 offset:26624\n"
        "ds_read_b64_tr_b16 a[74:75], v8 offset:27136\n"
        "s_mul_i32 s98, s99, 37\n"
        "s_lshr_b32 s98, s98, 8\n"
        "s_mul_i32 s98, s98, 7\n"
        "s_waitcnt lgkmcnt(7)\n"
        "v_mfma_f32_32x32x16_bf16 v[96:111], a[80:83], v[128:131], 0\n"
        "ds_read_b64_tr_b16 a[76:77], v9 offset:26624\n"
        "ds_read_b64_tr_b16 a[78:79], v9 offset:27136\n"
        "s_waitcnt lgkmcnt(4)\n"
        "s_barrier\n"
        "v_mfma_f32_16x16x32_bf16 a[104:107], a[96:99], a[80:83], 0\n"
        "ds_read_b128 v[120:123], v19 offset:0\n"
        "v_mov_b32 v117, 0\n"
        "v_mfma_f32_32x32x16_bf16 v[96:111], a[84:87], v[132:135], v[96:111]\n"
        "ds_read_b64_tr_b16 a[80:81], v6 offset:28672\n"
        "ds_read_b64_tr_b16 a[82:83], v6 offset:29184\n"
        "s_sub_u32 s98, s99, s98\n"
        "s_mul_i32 s98, s98, 4\n"
        "s_mov_b32 s20, 0x06543210\n"
        "v_mfma_f32_32x32x16_bf16 v[96:111], a[88:91], v[136:139], v[96:111]\n"
        "ds_read_b64_tr_b16 a[84:85], v7 offset:28672\n"
        "ds_read_b64_tr_b16 a[86:87], v7 offset:29184\n"
        "s_mov_b32 s21, 0x654321\n"
        "s_lshr_b64 s[20:21], s[20:21], s98\n"
        "s_lshl_b32 s71, s84, 4\n"
        "s_waitcnt lgkmcnt(4)\n"
        "v_mfma_f32_32x32x16_bf16 v[96:111], a[92:95], v[140:143], v[96:111]\n"
        "ds_read_b64_tr_b16 a[88:89], v8 offset:28672\n"
        "ds_read_b64_tr_b16 a[90:91], v8 offset:29184\n"
        "v_add_f32 v120, v120, v121\n"
        "v_add_f32 v122, v122, v123\n"
        "v_add_f32 v120, v120, v122\n"
        "v_cvt_pk_bf16_f32 v121, v120, 0\n"
        "s_waitcnt lgkmcnt(15)\n"
        "v_mfma_f32_32x32x16_bf16 v[96:111], a[64:67], v[144:147], v[96:111]\n"
        "ds_read_b64_tr_b16 a[92:93], v9 offset:28672\n"
        "ds_read_b64_tr_b16 a[94:95], v9 offset:29184\n"
        "v_lshlrev_b32 v121, 16, v121\n"
        "v_sub_f32 v121, v120, v121\n"
        "v_cvt_pk_bf16_f32 v116, v120, v121\n"
        "v_mfma_f32_16x16x32_bf16 a[104:107], a[96:99], a[64:67], a[104:107]\n"
        "s_add_u32 s71, s71, s80\n"
        "s_mul_i32 s70, s71, 32\n"
        "s_add_u32 s69, s70, 0x9000\n"
        "v_mfma_f32_32x32x16_bf16 v[64:79], v[116:119], v[112:115], v[64:79]\n"
        "s_lshr_b32 s98, s78, 1\n"
        "s_lshl_b32 s99, s85, 1\n"
        "s_add_u32 s99, s99, s98\n"
        "s_mul_i32 s98, s78, 28672\n"
        "s_add_u32 s98, s98, 0x500000\n"
        "s_add_u32 s18, s28, s98\n"
        "s_addc_u32 s19, s29, 0\n"
        "v_mfma_f32_32x32x16_bf16 v[96:111], a[68:71], v[148:151], v[96:111]\n"
        "ds_read_b64_tr_b16 a[64:65], v6 offset:30720\n"
        "ds_read_b64_tr_b16 a[66:67], v6 offset:31232\n"
        "s_cmp_lt_u32 s86, 7\n"
        "s_cselect_b32 s42, 0x700000, 0\n"
        "s_mov_b32 s43, 0x20000\n"
        "v_mfma_f32_32x32x16_bf16 v[96:111], a[72:75], v[152:155], v[96:111]\n"
        "ds_read_b64_tr_b16 a[68:69], v7 offset:30720\n"
        "ds_read_b64_tr_b16 a[70:71], v7 offset:31232\n"
        "v_mul_f32 v64, v27, v64\n"
        "v_mul_f32 v65, v27, v65\n"
        "v_cvt_pk_bf16_f32 v88, v64, v65\n"
        "v_mul_f32 v66, v27, v66\n"
        "v_mfma_f32_32x32x16_bf16 v[96:111], a[76:79], v[156:159], v[96:111]\n"
        "ds_read_b64_tr_b16 a[72:73], v8 offset:30720\n"
        "ds_read_b64_tr_b16 a[74:75], v8 offset:31232\n"
        "v_mul_f32 v67, v27, v67\n"
        "v_cvt_pk_bf16_f32 v89, v66, v67\n"
        "v_mul_f32 v68, v27, v68\n"
        "v_mul_f32 v69, v27, v69\n"
        "s_waitcnt lgkmcnt(6)\n"
        "v_mfma_f32_32x32x16_bf16 v[96:111], a[80:83], v[160:163], v[96:111]\n"
        "ds_read_b64_tr_b16 a[76:77], v9 offset:30720\n"
        "ds_read_b64_tr_b16 a[78:79], v9 offset:31232\n"
        "s_waitcnt vmcnt(1)\n"
        "v_cmp_ne_u64 vcc, s[88:89], v[250:251]\n"
        "s_nop 1\n"
        "s_cbranch_vccnz kv_spin_%=\n"
        "kv_ok_%=:\n"
        "v_mfma_f32_16x16x32_bf16 a[104:107], a[96:99], a[80:83], a[104:107]\n"
        "buffer_load_dwordx4 a[224:227], v28, s[12:15], 0 offen sc1\n"
        "v_cvt_pk_bf16_f32 v90, v68, v69\n"
        "v_mul_f32 v70, v27, v70\n"
        "v_mfma_f32_32x32x16_bf16 v[96:111], a[84:87], v[164:167], v[96:111]\n"
        "ds_read_b64_tr_b16 a[80:81], v6 offset:32768\n"
        "ds_read_b64_tr_b16 a[82:83], v6 offset:33280\n"
        "v_mul_f32 v71, v27, v71\n"
        "v_cvt_pk_bf16_f32 v91, v70, v71\n"
        "v_mul_f32 v72, v27, v72\n"
        "buffer_load_dwordx4 a[228:231], v28, s[12:15], 0 offen offset:1024 sc1\n"
        "v_mfma_f32_32x32x16_bf16 v[96:111], a[88:91], v[168:171], v[96:111]\n"
        "ds_read_b64_tr_b16 a[84:85], v7 offset:32768\n"
        "ds_read_b64_tr_b16 a[86:87], v7 offset:33280\n"
        "v_mul_f32 v73, v27, v73\n"
        "v_cvt_pk_bf16_f32 v92, v72, v73\n"
        "v_mul_f32 v74, v27, v74\n"
        "v_mul_f32 v75, v27, v75\n"
        "v_mfma_f32_32x32x16_bf16 v[96:111], a[92:95], v[172:175], v[96:111]\n"
        "ds_read_b64_tr_b16 a[88:89], v8 offset:32768\n"
        "ds_read_b64_tr_b16 a[90:91], v8 offset:33280\n"
        "buffer_load_dwordx4 a[232:235], v29, s[12:15], 0 offen sc1\n"
        "v_cvt_pk_bf16_f32 v93, v74, v75\n"
        "v_mul_f32 v76, v27, v76\n"
        "s_waitcnt lgkmcnt(6)\n"
        "v_mfma_f32_32x32x16_bf16 v[96:111], a[64:67], v[176:179], v[96:111]\n"
        "ds_read_b64_tr_b16 a[92:93], v9 offset:32768\n"
        "ds_read_b64_tr_b16 a[94:95], v9 offset:33280\n"
        "v_mul_f32 v77, v27, v77\n"
        "v_cvt_pk_bf16_f32 v94, v76, v77\n"
        "buffer_load_dwordx4 a[236:239], v29, s[12:15], 0 offen offset:1024 sc1\n"
        "s_add_u32 s62, s62, 0x10000\n"
        "v_mfma_f32_16x16x32_bf16 a[104:107], a[96:99], a[64:67], a[104:107]\n"
        "s_and_b32 s62, s62, 0xfffff\n"
        "s_or_b32 s12, s95, s62\n"
        "v_mul_f32 v78, v27, v78\n"
        "v_mul_f32 v79, v27, v79\n"
        "v_mfma_f32_32x32x16_bf16 v[96:111], a[68:71], v[180:183], v[96:111]\n"
        "ds_read_b64_tr_b16 a[64:65], v6 offset:34816\n"
        "ds_read_b64_tr_b16 a[66:67], v6 offset:35328\n"
        "buffer_load_dwordx4 a[240:243], v28, s[12:15], 0 offen sc1\n"
        "v_cvt_pk_bf16_f32 v95, v78, v79\n"
        "v_permlane32_swap_b32 v88, v90\n"
        "v_mfma_f32_32x32x16_bf16 v[96:111], a[72:75], v[184:187], v[96:111]\n"
        "ds_read_b64_tr_b16 a[68:69], v7 offset:34816\n"
        "ds_read_b64_tr_b16 a[70:71], v7 offset:35328\n"
        "v_permlane32_swap_b32 v89, v91\n"
        "v_permlane32_swap_b32 v92, v94\n"
        "v_permlane32_swap_b32 v93, v95\n"
        "buffer_load_dwordx4 a[244:247], v28, s[12:15], 0 offen offset:1024 sc1\n"
        "v_mfma_f32_32x32x16_bf16 v[96:111], a[76:79], v[188:191], v[96:111]\n"
        "ds_read_b64_tr_b16 a[72:73], v8 offset:34816\n"
        "ds_read_b64_tr_b16 a[74:75], v8 offset:35328\n"
        "v_cndmask_b32 v88, v88, 0, s[6:7]\n"
        "v_cndmask_b32 v89, v89, 0, s[6:7]\n"
        "v_cndmask_b32 v94, v94, 0, s[4:5]\n"
        "v_cndmask_b32 v95, v95, 0, s[4:5]\n"
        "s_waitcnt lgkmcnt(6)\n"
        "v_mfma_f32_32x32x16_bf16 v[96:111], a[80:83], v[192:195], v[96:111]\n"
        "ds_read_b64_tr_b16 a[76:77], v9 offset:34816\n"
        "ds_read_b64_tr_b16 a[78:79], v9 offset:35328\n"
        "buffer_load_dwordx4 a[248:251], v29, s[12:15], 0 offen sc1\n"
        "v_mfma_f32_16x16x32_bf16 a[104:107], a[96:99], a[80:83], a[104:107]\n"
        "v_mfma_f32_32x32x16_bf16 v[96:111], a[84:87], v[196:199], v[96:111]\n"
        "ds_read_b64_tr_b16 a[80:81], v6 offset:36864\n"
        "ds_read_b64_tr_b16 a[82:83], v6 offset:37376\n"
        "buffer_load_dwordx4 a[252:255], v29, s[12:15], 0 offen offset:1024 sc1\n"
        "v_bfe_u32 v22, v1, 4, 5\n"
        "s_lshl_b32 s96, s85, 5\n"
        "v_add_u32 v22, s96, v22\n"
        "v_mfma_f32_32x32x16_bf16 v[96:111], a[88:91], v[200:203], v[96:111]\n"
        "ds_read_b64_tr_b16 a[84:85], v7 offset:36864\n"
        "ds_read_b64_tr_b16 a[86:87], v7 offset:37376\n"
        "v_mul_u32_u24 v22, 32, v22\n"
        "v_add_u32 v22, 0x9000, v22\n"
        "s_bfe_u32 s60, s20, 0x40000\n"
        "v_mfma_f32_32x32x16_bf16 v[96:111], a[92:95], v[204:207], v[96:111]\n"
        "ds_read_b64_tr_b16 a[88:89], v8 offset:36864\n"
        "ds_read_b64_tr_b16 a[90:91], v8 offset:37376\n"
        "s_mul_i32 s60, s60, 57344\n"
        "s_add_u32 s60, s60, 28672\n"
        "s_waitcnt lgkmcnt(6)\n"
        "v_mfma_f32_32x32x16_bf16 v[96:111], a[64:67], v[208:211], v[96:111]\n"
        "ds_read_b64_tr_b16 a[92:93], v9 offset:36864\n"
        "ds_read_b64_tr_b16 a[94:95], v9 offset:37376\n"
        "s_add_u32 s44, s16, s60\n"
        "s_addc_u32 s45, s17, 0\n"
        "s_add_u32 m0, s83, 0x1000\n"
        "v_mfma_f32_16x16x32_bf16 a[104:107], a[96:99], a[64:67], a[104:107]\n"
        "s_nop 0\n"
        "buffer_load_dwordx4 v14, s[44:47], 0 offen lds\n"
        "v_mfma_f32_32x32x16_bf16 v[96:111], a[68:71], v[212:215], v[96:111]\n"
        "ds_read_b64_tr_b16 a[64:65], v10 offset:24576\n"
        "ds_read_b64_tr_b16 a[66:67], v10 offset:25088\n"
        "v_mfma_f32_32x32x16_bf16 v[96:111], a[72:75], v[216:219], v[96:111]\n"
        "ds_read_b64_tr_b16 a[68:69], v10 offset:26624\n"
        "ds_read_b64_tr_b16 a[70:71], v10 offset:27136\n"
        "v_mfma_f32_32x32x16_bf16 v[96:111], a[76:79], v[220:223], v[96:111]\n"
        "ds_read_b64_tr_b16 a[72:73], v10 offset:28672\n"
        "ds_read_b64_tr_b16 a[74:75], v10 offset:29184\n"
        "buffer_load_dwordx4 v15, s[44:47], 0 offen offset:1024 lds\n"
        "s_waitcnt lgkmcnt(6)\n"
        "v_mfma_f32_32x32x16_bf16 v[96:111], a[80:83], v[224:227], v[96:111]\n"
        "ds_read_b64_tr_b16 a[76:77], v10 offset:30720\n"
        "ds_read_b64_tr_b16 a[78:79], v10 offset:31232\n"
        "v_mfma_f32_16x16x32_bf16 a[104:107], a[96:99], a[80:83], a[104:107]\n"
        "v_mfma_f32_32x32x16_bf16 v[96:111], a[84:87], v[228:231], v[96:111]\n"
        "ds_read_b64_tr_b16 a[80:81], v10 offset:32768\n"
        "ds_read_b64_tr_b16 a[82:83], v10 offset:33280\n"
        "buffer_load_dwordx4 v16, s[44:47], 0 offen offset:2048 lds\n"
        "v_mfma_f32_32x32x16_bf16 v[96:111], a[88:91], v[232:235], v[96:111]\n"
        "ds_read_b64_tr_b16 a[84:85], v10 offset:34816\n"
        "ds_read_b64_tr_b16 a[86:87], v10 offset:35328\n"
        "s_nop 0\n"
        "ds_write_b32 v18, a104 offset:40960\n"
        "v_mfma_f32_32x32x16_bf16 v[96:111], a[92:95], v[236:239], v[96:111]\n"
        "ds_read_b64_tr_b16 a[88:89], v10 offset:36864\n"
        "ds_read_b64_tr_b16 a[90:91], v10 offset:37376\n"
        "s_waitcnt lgkmcnt(7)\n"
        "v_mfma_f32_32x32x16_bf16 v[48:63], a[64:67], v[128:131], 0\n"
        "ds_read_b64_tr_b16 a[92:93], v11 offset:32768\n"
        "ds_read_b64_tr_b16 a[94:95], v11 offset:33280\n"
        "s_waitcnt lgkmcnt(4)\n"
        "s_barrier\n"
        "v_mfma_f32_16x16x32_bf16 a[100:103], a[96:99], a[64:67], 0\n"
        "ds_read_b128 v[120:123], v19 offset:40960\n"
        "v_accvgpr_read_b32 v117, a110\n"
        "v_mfma_f32_32x32x16_bf16 v[48:63], a[68:71], v[144:147], v[48:63]\n"
        "ds_read_b64_tr_b16 a[64:65], v12 offset:32768\n"
        "ds_read_b64_tr_b16 a[66:67], v12 offset:33280\n"
        "buffer_load_dwordx4 v17, s[44:47], 0 offen offset:3072 lds\n"
        "s_bfe_u32 s60, s20, 0x40004\n"
        "v_mfma_f32_16x16x32_bf16 a[100:103], a[96:99], a[68:71], a[100:103]\n"
        "s_mul_i32 s60, s60, 57344\n"
        "s_add_u32 s44, s16, s60\n"
        "s_addc_u32 s45, s17, 0\n"
        "v_mfma_f32_32x32x16_bf16 v[48:63], a[72:75], v[160:163], v[48:63]\n"
        "ds_read_b64_tr_b16 a[68:69], v13 offset:32768\n"
        "ds_read_b64_tr_b16 a[70:71], v13 offset:33280\n"
        "s_waitcnt lgkmcnt(4)\n"
        "v_add_f32 v120, v120, v121\n"
        "v_add_f32 v122, v122, v123\n"
        "v_add_f32 v120, v120, v122\n"
        "v_mfma_f32_16x16x32_bf16 a[100:103], a[96:99], a[72:75], a[100:103]\n"
        "v_cvt_pk_bf16_f32 v121, v120, 0\n"
        "v_lshlrev_b32 v121, 16, v121\n"
        "v_sub_f32 v121, v120, v121\n"
        "v_cvt_pk_bf16_f32 v116, v120, v121\n"
        "v_mfma_f32_32x32x16_bf16 v[48:63], a[76:79], v[176:179], v[48:63]\n"
        "ds_read_b64_tr_b16 a[72:73], v11 offset:34816\n"
        "ds_read_b64_tr_b16 a[74:75], v11 offset:35328\n"
        "s_waitcnt vmcnt(8)\n"
        "s_add_u32 m0, s83, 0x2000\n"
        "s_nop 0\n"
        "v_mfma_f32_32x32x16_bf16 v[96:111], v[116:119], v[112:115], v[96:111]\n"
        "buffer_load_dwordx4 v14, s[44:47], 0 offen lds\n"
        "v_mfma_f32_32x32x16_bf16 v[32:47], a[224:227], v[88:91], 0\n"
        "v_mfma_f32_32x32x16_bf16 v[32:47], a[228:231], v[92:95], v[32:47]\n"
        "v_mfma_f32_16x16x32_bf16 a[100:103], a[96:99], a[76:79], a[100:103]\n"
        "buffer_load_dwordx4 v15, s[44:47], 0 offen offset:1024 lds\n"
        "s_waitcnt lgkmcnt(15)\n"
        "v_mfma_f32_32x32x16_bf16 v[48:63], a[80:83], v[192:195], v[48:63]\n"
        "ds_read_b64_tr_b16 a[76:77], v12 offset:34816\n"
        "ds_read_b64_tr_b16 a[78:79], v12 offset:35328\n"
        "v_mfma_f32_16x16x32_bf16 a[100:103], a[96:99], a[80:83], a[100:103]\n"
        "s_nop 4\n"
        "v_max3_f32 v20, v32, v33, v34\n"
        "v_max3_f32 v21, v35, v36, v37\n"
        "v_max3_f32 v20, v20, v38, v39\n"
        "v_max3_f32 v21, v21, v40, v41\n"
        "v_mfma_f32_32x32x16_bf16 v[48:63], a[84:87], v[208:211], v[48:63]\n"
        "ds_read_b64_tr_b16 a[80:81], v13 offset:34816\n"
        "ds_read_b64_tr_b16 a[82:83], v13 offset:35328\n"
        "v_max3_f32 v20, v20, v42, v43\n"
        "v_max3_f32 v21, v21, v44, v45\n"
        "v_max3_f32 v20, v20, v46, v47\n"
        "v_max_f32 v20, v20, v21\n"
        "v_mfma_f32_16x16x32_bf16 a[100:103], a[96:99], a[84:87], a[100:103]\n"
        "v_mov_b32 v21, v20\n"
        "v_add_u32 v31, s83, v1\n"
        "s_nop 0\n"
        "v_permlane32_swap_b32 v20, v21\n"
        "v_mfma_f32_32x32x16_bf16 v[48:63], a[88:91], v[224:227], v[48:63]\n"
        "ds_read_b64_tr_b16 a[84:85], v11 offset:24576\n"
        "ds_read_b64_tr_b16 a[86:87], v11 offset:25088\n"
        "v_max_f32 v20, v20, v21\n"
        "v_add_f32 v20, 0x42b00000, v20\n"
        "v_cvt_pk_bf16_f32 v21, -v20, 0\n"
        "v_lshlrev_b32 v21, 16, v21\n"
        "v_mfma_f32_16x16x32_bf16 a[100:103], a[96:99], a[88:91], a[100:103]\n"
        "v_sub_f32 v21, -v20, v21\n"
        "v_cvt_pk_bf16_f32 v124, -v20, v21\n"
        "v_and_b32 v20, s90, v124\n"
        "v_sub_f32 v21, v21, v20\n"
        "v_mfma_f32_32x32x16_bf16 v[48:63], a[92:95], v[196:199], v[48:63]\n"
        "ds_read_b64_tr_b16 a[88:89], v12 offset:24576\n"
        "ds_read_b64_tr_b16 a[90:91], v12 offset:25088\n"
        "v_cvt_pk_bf16_f32 v125, v21, 0\n"
        "ds_write_b128 v31, v[88:91] offset:0\n"
        "s_waitcnt lgkmcnt(7)\n"
        "v_mfma_f32_32x32x16_bf16 v[48:63], a[64:67], v[200:203], v[48:63]\n"
        "ds_read_b64_tr_b16 a[92:93], v13 offset:24576\n"
        "ds_read_b64_tr_b16 a[94:95], v13 offset:25088\n"
        "ds_write_b128 v31, v[92:95] offset:1024\n"
        "v_mfma_f32_32x32x16_bf16 v[48:63], a[68:71], v[204:207], v[48:63]\n"
        "ds_read_b64_tr_b16 a[64:65], v11 offset:26624\n"
        "ds_read_b64_tr_b16 a[66:67], v11 offset:27136\n"
        "ds_write_b32 v18, a100 offset:0\n"
        "v_mfma_f32_32x32x16_bf16 v[48:63], a[72:75], v[212:215], v[48:63]\n"
        "ds_read_b64_tr_b16 a[68:69], v12 offset:26624\n"
        "ds_read_b64_tr_b16 a[70:71], v12 offset:27136\n"
        "ds_write_b128 v31, v[124:127] offset:2048\n"
        "v_mfma_f32_32x32x16_bf16 v[48:63], a[76:79], v[216:219], v[48:63]\n"
        "ds_read_b64_tr_b16 a[72:73], v13 offset:26624\n"
        "ds_read_b64_tr_b16 a[74:75], v13 offset:27136\n"
        "s_waitcnt lgkmcnt(5)\n"
        "s_barrier\n"
        "s_waitcnt lgkmcnt(15)\n"
        "v_mfma_f32_32x32x16_bf16 v[48:63], a[80:83], v[220:223], v[48:63]\n"
        "ds_read_b64_tr_b16 a[76:77], v11 offset:28672\n"
        "ds_read_b64_tr_b16 a[78:79], v11 offset:29184\n"
        "ds_read_b128 v[120:123], v19 offset:0\n"
        "v_accvgpr_read_b32 v117, a111\n"
        "v_mfma_f32_32x32x16_bf16 v[48:63], a[84:87], v[132:135], v[48:63]\n"
        "ds_read_b64_tr_b16 a[80:81], v12 offset:28672\n"
        "ds_read_b64_tr_b16 a[82:83], v12 offset:29184\n"
        "buffer_load_dwordx4 v16, s[44:47], 0 offen offset:2048 lds\n"
        "v_mfma_f32_32x32x16_bf16 v[48:63], a[88:91], v[136:139], v[48:63]\n"
        "ds_read_b64_tr_b16 a[84:85], v13 offset:28672\n"
        "ds_read_b64_tr_b16 a[86:87], v13 offset:29184\n"
        "v_mfma_f32_32x32x16_bf16 v[48:63], a[92:95], v[140:143], v[48:63]\n"
        "ds_read_b64_tr_b16 a[88:89], v11 offset:30720\n"
        "ds_read_b64_tr_b16 a[90:91], v11 offset:31232\n"
        "s_waitcnt lgkmcnt(6)\n"
        "v_add_f32 v120, v120, v121\n"
        "v_add_f32 v122, v122, v123\n"
        "v_add_f32 v120, v120, v122\n"
        "s_waitcnt lgkmcnt(15)\n"
        "v_mfma_f32_32x32x16_bf16 v[48:63], a[64:67], v[148:151], v[48:63]\n"
        "ds_read_b64_tr_b16 a[92:93], v12 offset:30720\n"
        "ds_read_b64_tr_b16 a[94:95], v12 offset:31232\n"
        "v_cvt_pk_bf16_f32 v121, v120, 0\n"
        "v_lshlrev_b32 v121, 16, v121\n"
        "v_sub_f32 v121, v120, v121\n"
        "v_cvt_pk_bf16_f32 v116, v120, v121\n"
        "v_mfma_f32_32x32x16_bf16 v[48:63], a[68:71], v[152:155], v[48:63]\n"
        "ds_read_b64_tr_b16 a[64:65], v13 offset:30720\n"
        "ds_read_b64_tr_b16 a[66:67], v13 offset:31232\n"
        "buffer_load_dwordx4 v17, s[44:47], 0 offen offset:3072 lds\n"
        "s_add_u32 m0, s83, 0x3000\n"
        "v_mfma_f32_32x32x16_bf16 v[48:63], v[116:119], v[112:115], v[48:63]\n"
        "v_max_f32 v96, 0, v96\n"
        "v_max_f32 v97, 0, v97\n"
        "v_mul_f32 v96, v26, v96\n"
        "v_mul_f32 v97, v26, v97\n"
        "v_mul_f32 v96, v96, v96\n"
        "v_mul_f32 v97, v97, v97\n"
        "v_cvt_pk_bf16_f32 v80, v96, v97\n"
        "v_max_f32 v98, 0, v98\n"
        "v_mfma_f32_32x32x16_bf16 v[48:63], a[72:75], v[156:159], v[48:63]\n"
        "ds_read_b64_tr_b16 a[68:69], v11 offset:36864\n"
        "ds_read_b64_tr_b16 a[70:71], v11 offset:37376\n"
        "v_max_f32 v99, 0, v99\n"
        "v_mul_f32 v98, v26, v98\n"
        "v_mul_f32 v99, v26, v99\n"
        "v_mul_f32 v98, v98, v98\n"
        "v_mfma_f32_32x32x16_bf16 v[48:63], a[76:79], v[164:167], v[48:63]\n"
        "ds_read_b64_tr_b16 a[72:73], v12 offset:36864\n"
        "ds_read_b64_tr_b16 a[74:75], v12 offset:37376\n"
        "v_mul_f32 v99, v99, v99\n"
        "v_cvt_pk_bf16_f32 v81, v98, v99\n"
        "v_max_f32 v100, 0, v100\n"
        "v_max_f32 v101, 0, v101\n"
        "s_waitcnt lgkmcnt(6)\n"
        "v_mfma_f32_32x32x16_bf16 v[48:63], a[80:83], v[168:171], v[48:63]\n"
        "ds_read_b64_tr_b16 a[76:77], v13 offset:36864\n"
        "ds_read_b64_tr_b16 a[78:79], v13 offset:37376\n"
        "v_mul_f32 v100, v26, v100\n"
        "v_mul_f32 v101, v26, v101\n"
        "v_mul_f32 v100, v100, v100\n"
        "v_mul_f32 v101, v101, v101\n"
        "v_mfma_f32_32x32x16_bf16 v[48:63], a[84:87], v[172:175], v[48:63]\n"
        "v_cvt_pk_bf16_f32 v82, v100, v101\n"
        "v_max_f32 v102, 0, v102\n"
        "v_max_f32 v103, 0, v103\n"
        "v_mul_f32 v102, v26, v102\n"
        "v_mul_f32 v103, v26, v103\n"
        "v_mul_f32 v102, v102, v102\n"
        "v_mul_f32 v103, v103, v103\n"
        "v_cvt_pk_bf16_f32 v83, v102, v103\n"
        "v_mfma_f32_32x32x16_bf16 v[48:63], a[88:91], v[180:183], v[48:63]\n"
        "buffer_store_dwordx4 v[80:83], v1, s[36:39], s65 offen offset:0 sc1\n"
        "v_max_f32 v104, 0, v104\n"
        "v_max_f32 v105, 0, v105\n"
        "v_mul_f32 v104, v26, v104\n"
        "v_mul_f32 v105, v26, v105\n"
        "v_mul_f32 v104, v104, v104\n"
        "v_mul_f32 v105, v105, v105\n"
        "v_mfma_f32_32x32x16_bf16 v[48:63], a[92:95], v[184:187], v[48:63]\n"
        "v_cvt_pk_bf16_f32 v84, v104, v105\n"
        "v_max_f32 v106, 0, v106\n"
        "v_max_f32 v107, 0, v107\n"
        "v_mul_f32 v106, v26, v106\n"
        "v_mul_f32 v107, v26, v107\n"
        "v_mul_f32 v106, v106, v106\n"
        "v_mul_f32 v107, v107, v107\n"
        "v_cvt_pk_bf16_f32 v85, v106, v107\n"
        "s_waitcnt lgkmcnt(0)\n"
        "v_mfma_f32_32x32x16_bf16 v[48:63], a[64:67], v[188:191], v[48:63]\n"
        "v_max_f32 v108, 0, v108\n"
        "v_max_f32 v109, 0, v109\n"
        "v_mul_f32 v108, v26, v108\n"
        "v_mul_f32 v109, v26, v109\n"
        "v_mul_f32 v108, v108, v108\n"
        "v_mul_f32 v109, v109, v109\n"
        "v_cvt_pk_bf16_f32 v86, v108, v109\n"
        "v_max_f32 v110, 0, v110\n"
        "v_mfma_f32_32x32x16_bf16 v[48:63], a[68:71], v[228:231], v[48:63]\n"
        "v_max_f32 v111, 0, v111\n"
        "v_mul_f32 v110, v26, v110\n"
        "v_mul_f32 v111, v26, v111\n"
        "v_mul_f32 v110, v110, v110\n"
        "v_mul_f32 v111, v111, v111\n"
        "v_cvt_pk_bf16_f32 v87, v110, v111\n"
        "buffer_store_dwordx4 v[84:87], v1, s[36:39], s65 offen offset:1024 sc1\n"
        "s_add_u32 s44, s44, 0x7000\n"
        "s_addc_u32 s45, s45, 0\n"
        "buffer_load_dwordx4 v14, s[44:47], 0 offen lds\n"
        "buffer_load_dwordx4 v15, s[44:47], 0 offen offset:1024 lds\n"
        "buffer_load_dwordx4 v16, s[44:47], 0 offen offset:2048 lds\n"
        "buffer_load_dwordx4 v17, s[44:47], 0 offen offset:3072 lds\n"
        "v_add_u32 v6, s83, v1\n"
        "v_add_u32 v7, s94, v1\n"
        "v_add_u32 v8, s76, v1\n"
        "v_add_u32 v9, s77, v1\n"
        "s_waitcnt lgkmcnt(0)\n"
        "s_barrier\n"
        "ds_read_b128 v[36:39], v7 offset:2048\n"
        "ds_read_b128 v[40:43], v8 offset:2048\n"
        "ds_read_b128 v[44:47], v9 offset:2048\n"
        "ds_read_b128 v[32:35], v6 offset:2048\n"
        "ds_read_b128 v[200:203], v7 offset:0\n"
        "ds_read_b128 v[204:207], v7 offset:1024\n"
        "ds_read_b128 v[208:211], v8 offset:0\n"
        "ds_read_b128 v[212:215], v8 offset:1024\n"
        "ds_read_b128 v[216:219], v9 offset:0\n"
        "ds_read_b128 v[220:223], v9 offset:1024\n"
        "ds_read_b128 v[192:195], v6 offset:0\n"
        "ds_read_b128 v[196:199], v6 offset:1024\n"
        "v_mfma_f32_32x32x16_bf16 v[48:63], a[72:75], v[232:235], v[48:63]\n"
        "v_mfma_f32_32x32x16_bf16 v[48:63], a[76:79], v[236:239], v[48:63]\n"
        "s_waitcnt lgkmcnt(8)\n"
        "v_mfma_f32_32x32x16_bf16 v[144:159], v[252:255], v[36:39], 0\n"
        "s_mul_i32 s98, s99, 114688\n"
        "v_add_u32 v30, s98, v1\n"
        "s_xor_b32 s98, s99, 1\n"
        "s_mul_i32 s98, s98, 114688\n"
        "v_add_u32 v31, s98, v1\n"
        "v_mfma_f32_32x32x16_bf16 v[160:175], v[252:255], v[40:43], 0\n"
        "s_nop 3\n"
        "v_max_f32 v48, 0, v48\n"
        "v_max_f32 v49, 0, v49\n"
        "v_mul_f32 v48, v26, v48\n"
        "v_mul_f32 v49, v26, v49\n"
        "v_mul_f32 v48, v48, v48\n"
        "v_mul_f32 v49, v49, v49\n"
        "v_cvt_pk_bf16_f32 v80, v48, v49\n"
        "v_max_f32 v50, 0, v50\n"
        "v_max_f32 v51, 0, v51\n"
        "v_mfma_f32_32x32x16_bf16 v[176:191], v[252:255], v[44:47], 0\n"
        "v_mul_f32 v50, v26, v50\n"
        "v_mul_f32 v51, v26, v51\n"
        "v_mul_f32 v50, v50, v50\n"
        "v_mul_f32 v51, v51, v51\n"
        "v_cvt_pk_bf16_f32 v81, v50, v51\n"
        "v_max_f32 v52, 0, v52\n"
        "v_max_f32 v53, 0, v53\n"
        "v_mul_f32 v52, v26, v52\n"
        "v_mul_f32 v53, v26, v53\n"
        "v_mfma_f32_32x32x16_bf16 v[128:143], v[252:255], v[32:35], 0\n"
        "v_mul_f32 v52, v52, v52\n"
        "v_mul_f32 v53, v53, v53\n"
        "v_cvt_pk_bf16_f32 v82, v52, v53\n"
        "v_max_f32 v54, 0, v54\n"
        "v_max_f32 v55, 0, v55\n"
        "v_mul_f32 v54, v26, v54\n"
        "v_mul_f32 v55, v26, v55\n"
        "v_mul_f32 v54, v54, v54\n"
        "v_mul_f32 v55, v55, v55\n"
        "s_waitcnt vmcnt(63) lgkmcnt(0)\n"
        "v_mfma_f32_32x32x16_bf16 v[64:79], a[224:227], v[200:203], v[144:159]\n"
        "v_cvt_pk_bf16_f32 v83, v54, v55\n"
        "buffer_store_dwordx4 v[80:83], v1, s[36:39], s65 offen offset:2048 sc1\n"
        "v_max_f32 v56, 0, v56\n"
        "v_max_f32 v57, 0, v57\n"
        "v_mul_f32 v56, v26, v56\n"
        "v_mul_f32 v57, v26, v57\n"
        "v_mul_f32 v56, v56, v56\n"
        "v_mul_f32 v57, v57, v57\n"
        "v_mfma_f32_32x32x16_bf16 v[64:79], a[228:231], v[204:207], v[64:79]\n"
        "v_cvt_pk_bf16_f32 v84, v56, v57\n"
        "v_max_f32 v58, 0, v58\n"
        "v_max_f32 v59, 0, v59\n"
        "v_mul_f32 v58, v26, v58\n"
        "v_mul_f32 v59, v26, v59\n"
        "v_mul_f32 v58, v58, v58\n"
        "v_mul_f32 v59, v59, v59\n"
        "v_cvt_pk_bf16_f32 v85, v58, v59\n"
        "v_max_f32 v60, 0, v60\n"
        "v_max_f32 v61, 0, v61\n"
        "v_mul_f32 v60, v26, v60\n"
        "v_mul_f32 v61, v26, v61\n"
        "v_mul_f32 v60, v60, v60\n"
        "v_mul_f32 v61, v61, v61\n"
        "v_cvt_pk_bf16_f32 v86, v60, v61\n"
        "v_max_f32 v62, 0, v62\n"
        "v_max_f32 v63, 0, v63\n"
        "v_mul_f32 v62, v26, v62\n"
        "v_mul_f32 v63, v26, v63\n"
        "v_mul_f32 v62, v62, v62\n"
        "v_mul_f32 v63, v63, v63\n"
        "v_cvt_pk_bf16_f32 v87, v62, v63\n"
        "buffer_store_dwordx4 v[84:87], v1, s[36:39], s65 offen offset:3072 sc1\n"
        "v_mfma_f32_32x32x16_bf16 v[48:63], a[224:227], v[208:211], v[160:175]\n"
        "s_add_u32 s62, s62, 0x10000\n"
        "s_and_b32 s62, s62, 0xfffff\n"
        "v_exp_f32 v64, v64\n"
        "v_exp_f32 v65, v65\n"
        "v_exp_f32 v66, v66\n"
        "v_exp_f32 v67, v67\n"
        "v_cvt_pk_bf16_f32 v224, v64, v65\n"
        "v_cvt_pk_bf16_f32 v225, v66, v67\n"
        "s_or_b32 s12, s95, s62\n"
        "v_exp_f32 v68, v68\n"
        "v_exp_f32 v69, v69\n"
        "v_exp_f32 v70, v70\n"
        "v_exp_f32 v71, v71\n"
        "v_cvt_pk_bf16_f32 v226, v68, v69\n"
        "v_cvt_pk_bf16_f32 v227, v70, v71\n"
        "v_mfma_f32_32x32x16_bf16 v[48:63], a[228:231], v[212:215], v[48:63]\n"
        "buffer_load_dwordx4 v[32:35], v28, s[12:15], 0 offen sc1\n"
        "v_exp_f32 v72, v72\n"
        "v_exp_f32 v73, v73\n"
        "v_exp_f32 v74, v74\n"
        "v_exp_f32 v75, v75\n"
        "v_cvt_pk_bf16_f32 v228, v72, v73\n"
        "v_cvt_pk_bf16_f32 v229, v74, v75\n"
        "v_mfma_f32_32x32x16_bf16 v[96:111], a[232:235], v[224:227], 0\n"
        "buffer_load_dwordx4 v[36:39], v28, s[12:15], 0 offen offset:1024 sc1\n"
        "v_exp_f32 v76, v76\n"
        "v_exp_f32 v77, v77\n"
        "v_exp_f32 v78, v78\n"
        "v_exp_f32 v79, v79\n"
        "v_cvt_pk_bf16_f32 v230, v76, v77\n"
        "v_cvt_pk_bf16_f32 v231, v78, v79\n"
        "v_mfma_f32_32x32x16_bf16 v[64:79], a[224:227], v[216:219], v[176:191]\n"
        "buffer_load_dwordx4 v[40:43], v29, s[12:15], 0 offen sc1\n"
        "v_exp_f32 v48, v48\n"
        "v_exp_f32 v49, v49\n"
        "v_exp_f32 v50, v50\n"
        "v_exp_f32 v51, v51\n"
        "v_cvt_pk_bf16_f32 v224, v48, v49\n"
        "v_cvt_pk_bf16_f32 v225, v50, v51\n"
        "v_mfma_f32_32x32x16_bf16 v[96:111], a[236:239], v[228:231], v[96:111]\n"
        "s_barrier\n"
        "v_exp_f32 v52, v52\n"
        "v_exp_f32 v53, v53\n"
        "v_exp_f32 v54, v54\n"
        "v_exp_f32 v55, v55\n"
        "v_cvt_pk_bf16_f32 v226, v52, v53\n"
        "v_cvt_pk_bf16_f32 v227, v54, v55\n"
        "v_mfma_f32_32x32x16_bf16 v[64:79], a[228:231], v[220:223], v[64:79]\n"
        "buffer_load_dwordx4 v[44:47], v29, s[12:15], 0 offen offset:1024 sc1\n"
        "v_exp_f32 v56, v56\n"
        "v_exp_f32 v57, v57\n"
        "v_exp_f32 v58, v58\n"
        "v_exp_f32 v59, v59\n"
        "v_cvt_pk_bf16_f32 v228, v56, v57\n"
        "v_cvt_pk_bf16_f32 v229, v58, v59\n"
        "v_mfma_f32_32x32x16_bf16 v[112:127], a[232:235], v[224:227], 0\n"
        "s_bfe_u32 s60, s20, 0x40000\n"
        "v_exp_f32 v60, v60\n"
        "v_exp_f32 v61, v61\n"
        "v_exp_f32 v62, v62\n"
        "v_exp_f32 v63, v63\n"
        "v_cvt_pk_bf16_f32 v230, v60, v61\n"
        "v_cvt_pk_bf16_f32 v231, v62, v63\n"
        "v_mfma_f32_32x32x16_bf16 v[48:63], a[224:227], v[192:195], v[128:143]\n"
        "s_mul_i32 s60, s60, 57344\n"
        "v_exp_f32 v64, v64\n"
        "v_exp_f32 v65, v65\n"
        "v_exp_f32 v66, v66\n"
        "v_exp_f32 v67, v67\n"
        "v_cvt_pk_bf16_f32 v224, v64, v65\n"
        "v_cvt_pk_bf16_f32 v225, v66, v67\n"
        "v_mfma_f32_32x32x16_bf16 v[112:127], a[236:239], v[228:231], v[112:127]\n"
        "s_add_u32 s44, s16, s60\n"
        "s_addc_u32 s45, s17, 0\n"
        "v_exp_f32 v68, v68\n"
        "v_exp_f32 v69, v69\n"
        "v_exp_f32 v70, v70\n"
        "v_exp_f32 v71, v71\n"
        "v_cvt_pk_bf16_f32 v226, v68, v69\n"
        "v_cvt_pk_bf16_f32 v227, v70, v71\n"
        "v_mfma_f32_32x32x16_bf16 v[48:63], a[228:231], v[196:199], v[48:63]\n"
        "s_add_u32 m0, s83, 0x0\n"
        "v_exp_f32 v72, v72\n"
        "v_exp_f32 v73, v73\n"
        "v_exp_f32 v74, v74\n"
        "v_exp_f32 v75, v75\n"
        "v_cvt_pk_bf16_f32 v228, v72, v73\n"
        "v_cvt_pk_bf16_f32 v229, v74, v75\n"
        "v_mfma_f32_32x32x16_bf16 v[232:247], a[232:235], v[224:227], 0\n"
        "s_waitcnt vmcnt(20)\n"
        "v_exp_f32 v76, v76\n"
        "v_exp_f32 v77, v77\n"
        "v_exp_f32 v78, v78\n"
        "v_exp_f32 v79, v79\n"
        "v_cvt_pk_bf16_f32 v230, v76, v77\n"
        "v_cvt_pk_bf16_f32 v231, v78, v79\n"
        "v_mfma_f32_32x32x16_bf16 v[64:79], a[240:243], v[200:203], v[144:159]\n"
        "s_add_u32 s62, s62, 0x10000\n"
        "v_exp_f32 v48, v48\n"
        "v_exp_f32 v49, v49\n"
        "v_exp_f32 v50, v50\n"
        "v_exp_f32 v51, v51\n"
        "v_cvt_pk_bf16_f32 v224, v48, v49\n"
        "v_cvt_pk_bf16_f32 v225, v50, v51\n"
        "v_mfma_f32_32x32x16_bf16 v[232:247], a[236:239], v[228:231], v[232:247]\n"
        "s_and_b32 s62, s62, 0xfffff\n"
        "v_exp_f32 v52, v52\n"
        "v_exp_f32 v53, v53\n"
        "v_exp_f32 v54, v54\n"
        "v_exp_f32 v55, v55\n"
        "v_cvt_pk_bf16_f32 v226, v52, v53\n"
        "v_cvt_pk_bf16_f32 v227, v54, v55\n"
        "v_mfma_f32_32x32x16_bf16 v[64:79], a[244:247], v[204:207], v[64:79]\n"
        "s_or_b32 s12, s95, s62\n"
        "v_exp_f32 v56, v56\n"
        "v_exp_f32 v57, v57\n"
        "v_exp_f32 v58, v58\n"
        "v_exp_f32 v59, v59\n"
        "v_cvt_pk_bf16_f32 v228, v56, v57\n"
        "v_cvt_pk_bf16_f32 v229, v58, v59\n"
        "v_mfma_f32_32x32x16_bf16 v[80:95], a[232:235], v[224:227], 0\n"
        "buffer_load_dwordx4 v14, s[44:47], 0 offen lds\n"
        "v_exp_f32 v60, v60\n"
        "v_exp_f32 v61, v61\n"
        "v_exp_f32 v62, v62\n"
        "v_exp_f32 v63, v63\n"
        "v_cvt_pk_bf16_f32 v230, v60, v61\n"
        "v_cvt_pk_bf16_f32 v231, v62, v63\n"
        "v_mfma_f32_32x32x16_bf16 v[48:63], a[240:243], v[208:211], v[160:175]\n"
        "buffer_load_dwordx4 a[224:227], v28, s[12:15], 0 offen sc1\n"
        "v_exp_f32 v64, v64\n"
        "v_exp_f32 v65, v65\n"
        "v_exp_f32 v66, v66\n"
        "v_exp_f32 v67, v67\n"
        "v_cvt_pk_bf16_f32 v224, v64, v65\n"
        "v_cvt_pk_bf16_f32 v225, v66, v67\n"
        "v_mfma_f32_32x32x16_bf16 v[80:95], a[236:239], v[228:231], v[80:95]\n"
        "buffer_load_dwordx4 v15, s[44:47], 0 offen offset:1024 lds\n"
        "v_exp_f32 v68, v68\n"
        "v_exp_f32 v69, v69\n"
        "v_exp_f32 v70, v70\n"
        "v_exp_f32 v71, v71\n"
        "v_cvt_pk_bf16_f32 v226, v68, v69\n"
        "v_cvt_pk_bf16_f32 v227, v70, v71\n"
        "v_mfma_f32_32x32x16_bf16 v[48:63], a[244:247], v[212:215], v[48:63]\n"
        "buffer_load_dwordx4 a[228:231], v28, s[12:15], 0 offen offset:1024 sc1\n"
        "v_exp_f32 v72, v72\n"
        "v_exp_f32 v73, v73\n"
        "v_exp_f32 v74, v74\n"
        "v_exp_f32 v75, v75\n"
        "v_cvt_pk_bf16_f32 v228, v72, v73\n"
        "v_cvt_pk_bf16_f32 v229, v74, v75\n"
        "v_mfma_f32_32x32x16_bf16 v[96:111], a[248:251], v[224:227], v[96:111]\n"
        "buffer_load_dwordx4 v16, s[44:47], 0 offen offset:2048 lds\n"
        "v_exp_f32 v76, v76\n"
        "v_exp_f32 v77, v77\n"
        "v_exp_f32 v78, v78\n"
        "v_exp_f32 v79, v79\n"
        "v_cvt_pk_bf16_f32 v230, v76, v77\n"
        "v_cvt_pk_bf16_f32 v231, v78, v79\n"
        "v_mfma_f32_32x32x16_bf16 v[64:79], a[240:243], v[216:219], v[176:191]\n"
        "buffer_load_dwordx4 a[232:235], v29, s[12:15], 0 offen sc1\n"
        "v_exp_f32 v48, v48\n"
        "v_exp_f32 v49, v49\n"
        "v_exp_f32 v50, v50\n"
        "v_exp_f32 v51, v51\n"
        "v_cvt_pk_bf16_f32 v224, v48, v49\n"
        "v_cvt_pk_bf16_f32 v225, v50, v51\n"
        "v_mfma_f32_32x32x16_bf16 v[96:111], a[252:255], v[228:231], v[96:111]\n"
        "buffer_load_dwordx4 v17, s[44:47], 0 offen offset:3072 lds\n"
        "v_exp_f32 v52, v52\n"
        "v_exp_f32 v53, v53\n"
        "v_exp_f32 v54, v54\n"
        "v_exp_f32 v55, v55\n"
        "v_cvt_pk_bf16_f32 v226, v52, v53\n"
        "v_cvt_pk_bf16_f32 v227, v54, v55\n"
        "v_mfma_f32_32x32x16_bf16 v[64:79], a[244:247], v[220:223], v[64:79]\n"
        "buffer_load_dwordx4 a[236:239], v29, s[12:15], 0 offen offset:1024 sc1\n"
        "v_exp_f32 v56, v56\n"
        "v_exp_f32 v57, v57\n"
        "v_exp_f32 v58, v58\n"
        "v_exp_f32 v59, v59\n"
        "v_cvt_pk_bf16_f32 v228, v56, v57\n"
        "v_cvt_pk_bf16_f32 v229, v58, v59\n"
        "v_mfma_f32_32x32x16_bf16 v[112:127], a[248:251], v[224:227], v[112:127]\n"
        "s_bfe_u32 s60, s20, 0x40008\n"
        "v_exp_f32 v60, v60\n"
        "v_exp_f32 v61, v61\n"
        "v_exp_f32 v62, v62\n"
        "v_exp_f32 v63, v63\n"
        "v_cvt_pk_bf16_f32 v230, v60, v61\n"
        "v_cvt_pk_bf16_f32 v231, v62, v63\n"
        "v_mfma_f32_32x32x16_bf16 v[48:63], a[240:243], v[192:195], v[128:143]\n"
        "s_mul_i32 s60, s60, 57344\n"
        "v_exp_f32 v64, v64\n"
        "v_exp_f32 v65, v65\n"
        "v_exp_f32 v66, v66\n"
        "v_exp_f32 v67, v67\n"
        "v_cvt_pk_bf16_f32 v224, v64, v65\n"
        "v_cvt_pk_bf16_f32 v225, v66, v67\n"
        "v_mfma_f32_32x32x16_bf16 v[112:127], a[252:255], v[228:231], v[112:127]\n"
        "s_add_u32 s44, s16, s60\n"
        "s_addc_u32 s45, s17, 0\n"
        "v_exp_f32 v68, v68\n"
        "v_exp_f32 v69, v69\n"
        "v_exp_f32 v70, v70\n"
        "v_exp_f32 v71, v71\n"
        "v_cvt_pk_bf16_f32 v226, v68, v69\n"
        "v_cvt_pk_bf16_f32 v227, v70, v71\n"
        "v_mfma_f32_32x32x16_bf16 v[48:63], a[244:247], v[196:199], v[48:63]\n"
        "s_add_u32 m0, s83, 0x4000\n"
        "v_exp_f32 v72, v72\n"
        "v_exp_f32 v73, v73\n"
        "v_exp_f32 v74, v74\n"
        "v_exp_f32 v75, v75\n"
        "v_cvt_pk_bf16_f32 v228, v72, v73\n"
        "v_cvt_pk_bf16_f32 v229, v74, v75\n"
        "v_mfma_f32_32x32x16_bf16 v[232:247], a[248:251], v[224:227], v[232:247]\n"
        "s_waitcnt vmcnt(8)\n"
        "v_exp_f32 v76, v76\n"
        "v_exp_f32 v77, v77\n"
        "v_exp_f32 v78, v78\n"
        "v_exp_f32 v79, v79\n"
        "v_cvt_pk_bf16_f32 v230, v76, v77\n"
        "v_cvt_pk_bf16_f32 v231, v78, v79\n"
        "v_mfma_f32_32x32x16_bf16 v[64:79], v[32:35], v[200:203], v[144:159]\n"
        "s_add_u32 s62, s62, 0x10000\n"
        "v_exp_f32 v48, v48\n"
        "v_exp_f32 v49, v49\n"
        "v_exp_f32 v50, v50\n"
        "v_exp_f32 v51, v51\n"
        "v_cvt_pk_bf16_f32 v224, v48, v49\n"
        "v_cvt_pk_bf16_f32 v225, v50, v51\n"
        "v_mfma_f32_32x32x16_bf16 v[232:247], a[252:255], v[228:231], v[232:247]\n"
        "s_and_b32 s62, s62, 0xfffff\n"
        "v_exp_f32 v52, v52\n"
        "v_exp_f32 v53, v53\n"
        "v_exp_f32 v54, v54\n"
        "v_exp_f32 v55, v55\n"
        "v_cvt_pk_bf16_f32 v226, v52, v53\n"
        "v_cvt_pk_bf16_f32 v227, v54, v55\n"
        "v_mfma_f32_32x32x16_bf16 v[64:79], v[36:39], v[204:207], v[64:79]\n"
        "s_or_b32 s12, s95, s62\n"
        "v_exp_f32 v56, v56\n"
        "v_exp_f32 v57, v57\n"
        "v_exp_f32 v58, v58\n"
        "v_exp_f32 v59, v59\n"
        "v_cvt_pk_bf16_f32 v228, v56, v57\n"
        "v_cvt_pk_bf16_f32 v229, v58, v59\n"
        "v_mfma_f32_32x32x16_bf16 v[80:95], a[248:251], v[224:227], v[80:95]\n"
        "s_waitcnt vmcnt(63)\n"
        "buffer_store_dwordx2 v[24:25], v23, s[28:31], s69 offen sc1\n"
        "v_exp_f32 v60, v60\n"
        "v_exp_f32 v61, v61\n"
        "v_exp_f32 v62, v62\n"
        "v_exp_f32 v63, v63\n"
        "v_cvt_pk_bf16_f32 v230, v60, v61\n"
        "v_cvt_pk_bf16_f32 v231, v62, v63\n"
        "v_mfma_f32_32x32x16_bf16 v[48:63], v[32:35], v[208:211], v[160:175]\n"
        "buffer_load_dwordx4 a[240:243], v28, s[12:15], 0 offen sc1\n"
        "v_exp_f32 v64, v64\n"
        "v_exp_f32 v65, v65\n"
        "v_exp_f32 v66, v66\n"
        "v_exp_f32 v67, v67\n"
        "v_cvt_pk_bf16_f32 v224, v64, v65\n"
        "v_cvt_pk_bf16_f32 v225, v66, v67\n"
        "v_mfma_f32_32x32x16_bf16 v[80:95], a[252:255], v[228:231], v[80:95]\n"
        "buffer_load_dwordx4 v14, s[44:47], 0 offen lds\n"
        "v_exp_f32 v68, v68\n"
        "v_exp_f32 v69, v69\n"
        "v_exp_f32 v70, v70\n"
        "v_exp_f32 v71, v71\n"
        "v_cvt_pk_bf16_f32 v226, v68, v69\n"
        "v_cvt_pk_bf16_f32 v227, v70, v71\n"
        "v_mfma_f32_32x32x16_bf16 v[48:63], v[36:39], v[212:215], v[48:63]\n"
        "buffer_load_dwordx4 a[244:247], v28, s[12:15], 0 offen offset:1024 sc1\n"
        "v_exp_f32 v72, v72\n"
        "v_exp_f32 v73, v73\n"
        "v_exp_f32 v74, v74\n"
        "v_exp_f32 v75, v75\n"
        "v_cvt_pk_bf16_f32 v228, v72, v73\n"
        "v_cvt_pk_bf16_f32 v229, v74, v75\n"
        "v_mfma_f32_32x32x16_bf16 v[96:111], v[40:43], v[224:227], v[96:111]\n"
        "buffer_load_dwordx4 v15, s[44:47], 0 offen offset:1024 lds\n"
        "v_exp_f32 v76, v76\n"
        "v_exp_f32 v77, v77\n"
        "v_exp_f32 v78, v78\n"
        "v_exp_f32 v79, v79\n"
        "v_cvt_pk_bf16_f32 v230, v76, v77\n"
        "v_cvt_pk_bf16_f32 v231, v78, v79\n"
        "v_mfma_f32_32x32x16_bf16 v[64:79], v[32:35], v[216:219], v[176:191]\n"
        "buffer_load_dwordx4 a[248:251], v29, s[12:15], 0 offen sc1\n"
        "v_exp_f32 v48, v48\n"
        "v_exp_f32 v49, v49\n"
        "v_exp_f32 v50, v50\n"
        "v_exp_f32 v51, v51\n"
        "v_cvt_pk_bf16_f32 v224, v48, v49\n"
        "v_cvt_pk_bf16_f32 v225, v50, v51\n"
        "v_mfma_f32_32x32x16_bf16 v[96:111], v[44:47], v[228:231], v[96:111]\n"
        "buffer_load_dwordx4 v16, s[44:47], 0 offen offset:2048 lds\n"
        "v_exp_f32 v52, v52\n"
        "v_exp_f32 v53, v53\n"
        "v_exp_f32 v54, v54\n"
        "v_exp_f32 v55, v55\n"
        "v_cvt_pk_bf16_f32 v226, v52, v53\n"
        "v_cvt_pk_bf16_f32 v227, v54, v55\n"
        "v_mfma_f32_32x32x16_bf16 v[64:79], v[36:39], v[220:223], v[64:79]\n"
        "buffer_load_dwordx4 a[252:255], v29, s[12:15], 0 offen offset:1024 sc1\n"
        "v_exp_f32 v56, v56\n"
        "v_exp_f32 v57, v57\n"
        "v_exp_f32 v58, v58\n"
        "v_exp_f32 v59, v59\n"
        "v_cvt_pk_bf16_f32 v228, v56, v57\n"
        "v_cvt_pk_bf16_f32 v229, v58, v59\n"
        "v_mfma_f32_32x32x16_bf16 v[112:127], v[40:43], v[224:227], v[112:127]\n"
        "buffer_load_dwordx4 v17, s[44:47], 0 offen offset:3072 lds\n"
        "v_exp_f32 v60, v60\n"
        "v_exp_f32 v61, v61\n"
        "v_exp_f32 v62, v62\n"
        "v_exp_f32 v63, v63\n"
        "v_cvt_pk_bf16_f32 v230, v60, v61\n"
        "v_cvt_pk_bf16_f32 v231, v62, v63\n"
        "v_mfma_f32_32x32x16_bf16 v[48:63], v[32:35], v[192:195], v[128:143]\n"
        "s_add_u32 m0, s83, 0x5000\n"
        "v_exp_f32 v64, v64\n"
        "v_exp_f32 v65, v65\n"
        "v_exp_f32 v66, v66\n"
        "v_exp_f32 v67, v67\n"
        "v_cvt_pk_bf16_f32 v224, v64, v65\n"
        "v_cvt_pk_bf16_f32 v225, v66, v67\n"
        "v_mfma_f32_32x32x16_bf16 v[112:127], v[44:47], v[228:231], v[112:127]\n"
        "s_add_u32 s44, s44, 0x7000\n"
        "s_addc_u32 s45, s45, 0\n"
        "v_exp_f32 v68, v68\n"
        "v_exp_f32 v69, v69\n"
        "v_exp_f32 v70, v70\n"
        "v_exp_f32 v71, v71\n"
        "v_cvt_pk_bf16_f32 v226, v68, v69\n"
        "v_cvt_pk_bf16_f32 v227, v70, v71\n"
        "v_mfma_f32_32x32x16_bf16 v[48:63], v[36:39], v[196:199], v[48:63]\n"
        "buffer_load_dwordx4 v14, s[44:47], 0 offen lds\n"
        "v_exp_f32 v72, v72\n"
        "v_exp_f32 v73, v73\n"
        "v_exp_f32 v74, v74\n"
        "v_exp_f32 v75, v75\n"
        "v_cvt_pk_bf16_f32 v228, v72, v73\n"
        "v_cvt_pk_bf16_f32 v229, v74, v75\n"
        "v_mfma_f32_32x32x16_bf16 v[232:247], v[40:43], v[224:227], v[232:247]\n"
        "s_waitcnt vmcnt(10)\n"
        "v_exp_f32 v76, v76\n"
        "v_exp_f32 v77, v77\n"
        "v_exp_f32 v78, v78\n"
        "v_exp_f32 v79, v79\n"
        "v_cvt_pk_bf16_f32 v230, v76, v77\n"
        "v_cvt_pk_bf16_f32 v231, v78, v79\n"
        "v_mfma_f32_32x32x16_bf16 v[64:79], a[224:227], v[200:203], v[144:159]\n"
        "s_add_u32 s62, s62, 0x10000\n"
        "v_exp_f32 v48, v48\n"
        "v_exp_f32 v49, v49\n"
        "v_exp_f32 v50, v50\n"
        "v_exp_f32 v51, v51\n"
        "v_cvt_pk_bf16_f32 v224, v48, v49\n"
        "v_cvt_pk_bf16_f32 v225, v50, v51\n"
        "v_mfma_f32_32x32x16_bf16 v[232:247], v[44:47], v[228:231], v[232:247]\n"
        "s_and_b32 s62, s62, 0xfffff\n"
        "v_exp_f32 v52, v52\n"
        "v_exp_f32 v53, v53\n"
        "v_exp_f32 v54, v54\n"
        "v_exp_f32 v55, v55\n"
        "v_cvt_pk_bf16_f32 v226, v52, v53\n"
        "v_cvt_pk_bf16_f32 v227, v54, v55\n"
        "v_mfma_f32_32x32x16_bf16 v[64:79], a[228:231], v[204:207], v[64:79]\n"
        "s_or_b32 s12, s95, s62\n"
        "v_exp_f32 v56, v56\n"
        "v_exp_f32 v57, v57\n"
        "v_exp_f32 v58, v58\n"
        "v_exp_f32 v59, v59\n"
        "v_cvt_pk_bf16_f32 v228, v56, v57\n"
        "v_cvt_pk_bf16_f32 v229, v58, v59\n"
        "v_mfma_f32_32x32x16_bf16 v[80:95], v[40:43], v[224:227], v[80:95]\n"
        "buffer_load_dwordx4 v15, s[44:47], 0 offen offset:1024 lds\n"
        "v_exp_f32 v60, v60\n"
        "v_exp_f32 v61, v61\n"
        "v_exp_f32 v62, v62\n"
        "v_exp_f32 v63, v63\n"
        "v_cvt_pk_bf16_f32 v230, v60, v61\n"
        "v_cvt_pk_bf16_f32 v231, v62, v63\n"
        "v_mfma_f32_32x32x16_bf16 v[48:63], a[224:227], v[208:211], v[160:175]\n"
        "buffer_load_dwordx4 v[32:35], v28, s[12:15], 0 offen sc1\n"
        "v_exp_f32 v64, v64\n"
        "v_exp_f32 v65, v65\n"
        "v_exp_f32 v66, v66\n"
        "v_exp_f32 v67, v67\n"
        "v_cvt_pk_bf16_f32 v224, v64, v65\n"
        "v_cvt_pk_bf16_f32 v225, v66, v67\n"
        "v_mfma_f32_32x32x16_bf16 v[80:95], v[44:47], v[228:231], v[80:95]\n"
        "buffer_load_dwordx4 v16, s[44:47], 0 offen offset:2048 lds\n"
        "v_exp_f32 v68, v68\n"
        "v_exp_f32 v69, v69\n"
        "v_exp_f32 v70, v70\n"
        "v_exp_f32 v71, v71\n"
        "v_cvt_pk_bf16_f32 v226, v68, v69\n"
        "v_cvt_pk_bf16_f32 v227, v70, v71\n"
        "v_mfma_f32_32x32x16_bf16 v[48:63], a[228:231], v[212:215], v[48:63]\n"
        "buffer_load_dwordx4 v[36:39], v28, s[12:15], 0 offen offset:1024 sc1\n"
        "v_exp_f32 v72, v72\n"
        "v_exp_f32 v73, v73\n"
        "v_exp_f32 v74, v74\n"
        "v_exp_f32 v75, v75\n"
        "v_cvt_pk_bf16_f32 v228, v72, v73\n"
        "v_cvt_pk_bf16_f32 v229, v74, v75\n"
        "v_mfma_f32_32x32x16_bf16 v[96:111], a[232:235], v[224:227], v[96:111]\n"
        "buffer_load_dwordx4 v17, s[44:47], 0 offen offset:3072 lds\n"
        "v_exp_f32 v76, v76\n"
        "v_exp_f32 v77, v77\n"
        "v_exp_f32 v78, v78\n"
        "v_exp_f32 v79, v79\n"
        "v_cvt_pk_bf16_f32 v230, v76, v77\n"
        "v_cvt_pk_bf16_f32 v231, v78, v79\n"
        "v_mfma_f32_32x32x16_bf16 v[64:79], a[224:227], v[216:219], v[176:191]\n"
        "buffer_load_dwordx4 v[40:43], v29, s[12:15], 0 offen sc1\n"
        "v_exp_f32 v48, v48\n"
        "v_exp_f32 v49, v49\n"
        "v_exp_f32 v50, v50\n"
        "v_exp_f32 v51, v51\n"
        "v_cvt_pk_bf16_f32 v224, v48, v49\n"
        "v_cvt_pk_bf16_f32 v225, v50, v51\n"
        "v_mfma_f32_32x32x16_bf16 v[96:111], a[236:239], v[228:231], v[96:111]\n"
        "s_bfe_u32 s60, s20, 0x4000c\n"
        "v_exp_f32 v52, v52\n"
        "v_exp_f32 v53, v53\n"
        "v_exp_f32 v54, v54\n"
        "v_exp_f32 v55, v55\n"
        "v_cvt_pk_bf16_f32 v226, v52, v53\n"
        "v_cvt_pk_bf16_f32 v227, v54, v55\n"
        "v_mfma_f32_32x32x16_bf16 v[64:79], a[228:231], v[220:223], v[64:79]\n"
        "buffer_load_dwordx4 v[44:47], v29, s[12:15], 0 offen offset:1024 sc1\n"
        "v_exp_f32 v56, v56\n"
        "v_exp_f32 v57, v57\n"
        "v_exp_f32 v58, v58\n"
        "v_exp_f32 v59, v59\n"
        "v_cvt_pk_bf16_f32 v228, v56, v57\n"
        "v_cvt_pk_bf16_f32 v229, v58, v59\n"
        "v_mfma_f32_32x32x16_bf16 v[112:127], a[232:235], v[224:227], v[112:127]\n"
        "s_mul_i32 s60, s60, 57344\n"
        "v_exp_f32 v60, v60\n"
        "v_exp_f32 v61, v61\n"
        "v_exp_f32 v62, v62\n"
        "v_exp_f32 v63, v63\n"
        "v_cvt_pk_bf16_f32 v230, v60, v61\n"
        "v_cvt_pk_bf16_f32 v231, v62, v63\n"
        "v_mfma_f32_32x32x16_bf16 v[48:63], a[224:227], v[192:195], v[128:143]\n"
        "buffer_load_dwordx2 v[250:251], v22, s[28:31], 0 offen sc1\n"
        "v_exp_f32 v64, v64\n"
        "v_exp_f32 v65, v65\n"
        "v_exp_f32 v66, v66\n"
        "v_exp_f32 v67, v67\n"
        "v_cvt_pk_bf16_f32 v224, v64, v65\n"
        "v_cvt_pk_bf16_f32 v225, v66, v67\n"
        "v_mfma_f32_32x32x16_bf16 v[112:127], a[236:239], v[228:231], v[112:127]\n"
        "s_add_u32 s44, s16, s60\n"
        "s_addc_u32 s45, s17, 0\n"
        "v_exp_f32 v68, v68\n"
        "v_exp_f32 v69, v69\n"
        "v_exp_f32 v70, v70\n"
        "v_exp_f32 v71, v71\n"
        "v_cvt_pk_bf16_f32 v226, v68, v69\n"
        "v_cvt_pk_bf16_f32 v227, v70, v71\n"
        "v_mfma_f32_32x32x16_bf16 v[48:63], a[228:231], v[196:199], v[48:63]\n"
        "s_add_u32 m0, s83, 0x6000\n"
        "v_exp_f32 v72, v72\n"
        "v_exp_f32 v73, v73\n"
        "v_exp_f32 v74, v74\n"
        "v_exp_f32 v75, v75\n"
        "v_cvt_pk_bf16_f32 v228, v72, v73\n"
        "v_cvt_pk_bf16_f32 v229, v74, v75\n"
        "v_mfma_f32_32x32x16_bf16 v[232:247], a[232:235], v[224:227], v[232:247]\n"
        "s_waitcnt vmcnt(10)\n"
        "v_exp_f32 v76, v76\n"
        "v_exp_f32 v77, v77\n"
        "v_exp_f32 v78, v78\n"
        "v_exp_f32 v79, v79\n"
        "v_cvt_pk_bf16_f32 v230, v76, v77\n"
        "v_cvt_pk_bf16_f32 v231, v78, v79\n"
        "v_mfma_f32_32x32x16_bf16 v[64:79], a[240:243], v[200:203], v[144:159]\n"
        "s_add_u32 s62, s62, 0x10000\n"
        "v_exp_f32 v48, v48\n"
        "v_exp_f32 v49, v49\n"
        "v_exp_f32 v50, v50\n"
        "v_exp_f32 v51, v51\n"
        "v_cvt_pk_bf16_f32 v224, v48, v49\n"
        "v_cvt_pk_bf16_f32 v225, v50, v51\n"
        "v_mfma_f32_32x32x16_bf16 v[232:247], a[236:239], v[228:231], v[232:247]\n"
        "s_and_b32 s62, s62, 0xfffff\n"
        "v_exp_f32 v52, v52\n"
        "v_exp_f32 v53, v53\n"
        "v_exp_f32 v54, v54\n"
        "v_exp_f32 v55, v55\n"
        "v_cvt_pk_bf16_f32 v226, v52, v53\n"
        "v_cvt_pk_bf16_f32 v227, v54, v55\n"
        "v_mfma_f32_32x32x16_bf16 v[64:79], a[244:247], v[204:207], v[64:79]\n"
        "s_or_b32 s12, s95, s62\n"
        "v_exp_f32 v56, v56\n"
        "v_exp_f32 v57, v57\n"
        "v_exp_f32 v58, v58\n"
        "v_exp_f32 v59, v59\n"
        "v_cvt_pk_bf16_f32 v228, v56, v57\n"
        "v_cvt_pk_bf16_f32 v229, v58, v59\n"
        "v_mfma_f32_32x32x16_bf16 v[80:95], a[232:235], v[224:227], v[80:95]\n"
        "buffer_load_dwordx4 v14, s[44:47], 0 offen lds\n"
        "v_exp_f32 v60, v60\n"
        "v_exp_f32 v61, v61\n"
        "v_exp_f32 v62, v62\n"
        "v_exp_f32 v63, v63\n"
        "v_cvt_pk_bf16_f32 v230, v60, v61\n"
        "v_cvt_pk_bf16_f32 v231, v62, v63\n"
        "v_mfma_f32_32x32x16_bf16 v[48:63], a[240:243], v[208:211], v[160:175]\n"
        "buffer_load_dwordx4 a[224:227], v28, s[12:15], 0 offen sc1\n"
        "v_exp_f32 v64, v64\n"
        "v_exp_f32 v65, v65\n"
        "v_exp_f32 v66, v66\n"
        "v_exp_f32 v67, v67\n"
        "v_cvt_pk_bf16_f32 v224, v64, v65\n"
        "v_cvt_pk_bf16_f32 v225, v66, v67\n"
        "v_mfma_f32_32x32x16_bf16 v[80:95], a[236:239], v[228:231], v[80:95]\n"
        "buffer_load_dwordx4 v15, s[44:47], 0 offen offset:1024 lds\n"
        "v_exp_f32 v68, v68\n"
        "v_exp_f32 v69, v69\n"
        "v_exp_f32 v70, v70\n"
        "v_exp_f32 v71, v71\n"
        "v_cvt_pk_bf16_f32 v226, v68, v69\n"
        "v_cvt_pk_bf16_f32 v227, v70, v71\n"
        "v_mfma_f32_32x32x16_bf16 v[48:63], a[244:247], v[212:215], v[48:63]\n"
        "buffer_load_dwordx4 a[228:231], v28, s[12:15], 0 offen offset:1024 sc1\n"
        "v_exp_f32 v72, v72\n"
        "v_exp_f32 v73, v73\n"
        "v_exp_f32 v74, v74\n"
        "v_exp_f32 v75, v75\n"
        "v_cvt_pk_bf16_f32 v228, v72, v73\n"
        "v_cvt_pk_bf16_f32 v229, v74, v75\n"
        "v_mfma_f32_32x32x16_bf16 v[96:111], a[248:251], v[224:227], v[96:111]\n"
        "buffer_load_dwordx4 v16, s[44:47], 0 offen offset:2048 lds\n"
        "v_exp_f32 v76, v76\n"
        "v_exp_f32 v77, v77\n"
        "v_exp_f32 v78, v78\n"
        "v_exp_f32 v79, v79\n"
        "v_cvt_pk_bf16_f32 v230, v76, v77\n"
        "v_cvt_pk_bf16_f32 v231, v78, v79\n"
        "v_mfma_f32_32x32x16_bf16 v[64:79], a[240:243], v[216:219], v[176:191]\n"
        "buffer_load_dwordx4 a[232:235], v29, s[12:15], 0 offen sc1\n"
        "v_exp_f32 v48, v48\n"
        "v_exp_f32 v49, v49\n"
        "v_exp_f32 v50, v50\n"
        "v_exp_f32 v51, v51\n"
        "v_cvt_pk_bf16_f32 v224, v48, v49\n"
        "v_cvt_pk_bf16_f32 v225, v50, v51\n"
        "v_mfma_f32_32x32x16_bf16 v[96:111], a[252:255], v[228:231], v[96:111]\n"
        "buffer_load_dwordx4 v17, s[44:47], 0 offen offset:3072 lds\n"
        "v_exp_f32 v52, v52\n"
        "v_exp_f32 v53, v53\n"
        "v_exp_f32 v54, v54\n"
        "v_exp_f32 v55, v55\n"
        "v_cvt_pk_bf16_f32 v226, v52, v53\n"
        "v_cvt_pk_bf16_f32 v227, v54, v55\n"
        "v_mfma_f32_32x32x16_bf16 v[64:79], a[244:247], v[220:223], v[64:79]\n"
        "buffer_load_dwordx4 a[236:239], v29, s[12:15], 0 offen offset:1024 sc1\n"
        "v_exp_f32 v56, v56\n"
        "v_exp_f32 v57, v57\n"
        "v_exp_f32 v58, v58\n"
        "v_exp_f32 v59, v59\n"
        "v_cvt_pk_bf16_f32 v228, v56, v57\n"
        "v_cvt_pk_bf16_f32 v229, v58, v59\n"
        "v_mfma_f32_32x32x16_bf16 v[112:127], a[248:251], v[224:227], v[112:127]\n"
        "s_add_u32 s66, s85, s86\n"
        "v_exp_f32 v60, v60\n"
        "v_exp_f32 v61, v61\n"
        "v_exp_f32 v62, v62\n"
        "v_exp_f32 v63, v63\n"
        "v_cvt_pk_bf16_f32 v230, v60, v61\n"
        "v_cvt_pk_bf16_f32 v231, v62, v63\n"
        "v_mfma_f32_32x32x16_bf16 v[48:63], a[240:243], v[192:195], v[128:143]\n"
        "s_mul_i32 s66, s66, 1\n"
        "v_exp_f32 v64, v64\n"
        "v_exp_f32 v65, v65\n"
        "v_exp_f32 v66, v66\n"
        "v_exp_f32 v67, v67\n"
        "v_cvt_pk_bf16_f32 v224, v64, v65\n"
        "v_cvt_pk_bf16_f32 v225, v66, v67\n"
        "v_mfma_f32_32x32x16_bf16 v[112:127], a[252:255], v[228:231], v[112:127]\n"
        "s_add_u32 s70, s70, 0x11000\n"
        "v_exp_f32 v68, v68\n"
        "v_exp_f32 v69, v69\n"
        "v_exp_f32 v70, v70\n"
        "v_exp_f32 v71, v71\n"
        "v_cvt_pk_bf16_f32 v226, v68, v69\n"
        "v_cvt_pk_bf16_f32 v227, v70, v71\n"
        "v_mfma_f32_32x32x16_bf16 v[48:63], a[244:247], v[196:199], v[48:63]\n"
        "s_lshl_b32 s71, s71, 11\n"
        "v_exp_f32 v72, v72\n"
        "v_exp_f32 v73, v73\n"
        "v_exp_f32 v74, v74\n"
        "v_exp_f32 v75, v75\n"
        "v_cvt_pk_bf16_f32 v228, v72, v73\n"
        "v_cvt_pk_bf16_f32 v229, v74, v75\n"
        "v_mfma_f32_32x32x16_bf16 v[232:247], a[248:251], v[224:227], v[232:247]\n"
        "s_waitcnt vmcnt(9)\n"
        "v_exp_f32 v76, v76\n"
        "v_exp_f32 v77, v77\n"
        "v_exp_f32 v78, v78\n"
        "v_exp_f32 v79, v79\n"
        "v_cvt_pk_bf16_f32 v230, v76, v77\n"
        "v_cvt_pk_bf16_f32 v231, v78, v79\n"
        "v_mfma_f32_32x32x16_bf16 v[64:79], v[32:35], v[200:203], v[144:159]\n"
        "s_add_u32 s62, s62, 0x10000\n"
        "v_exp_f32 v48, v48\n"
        "v_exp_f32 v49, v49\n"
        "v_exp_f32 v50, v50\n"
        "v_exp_f32 v51, v51\n"
        "v_cvt_pk_bf16_f32 v224, v48, v49\n"
        "v_cvt_pk_bf16_f32 v225, v50, v51\n"
        "v_mfma_f32_32x32x16_bf16 v[232:247], a[252:255], v[228:231], v[232:247]\n"
        "s_and_b32 s62, s62, 0xfffff\n"
        "v_exp_f32 v52, v52\n"
        "v_exp_f32 v53, v53\n"
        "v_exp_f32 v54, v54\n"
        "v_exp_f32 v55, v55\n"
        "v_cvt_pk_bf16_f32 v226, v52, v53\n"
        "v_cvt_pk_bf16_f32 v227, v54, v55\n"
        "v_mfma_f32_32x32x16_bf16 v[64:79], v[36:39], v[204:207], v[64:79]\n"
        "s_or_b32 s12, s95, s62\n"
        "v_exp_f32 v56, v56\n"
        "v_exp_f32 v57, v57\n"
        "v_exp_f32 v58, v58\n"
        "v_exp_f32 v59, v59\n"
        "v_cvt_pk_bf16_f32 v228, v56, v57\n"
        "v_cvt_pk_bf16_f32 v229, v58, v59\n"
        "v_mfma_f32_32x32x16_bf16 v[80:95], a[248:251], v[224:227], v[80:95]\n"
        "s_waitcnt vmcnt(8)\n"
        "v_cmp_ne_u64 vcc, s[88:89], v[250:251]\n"
        "s_nop 1\n"
        "s_cbranch_vccnz h_spin_%=\n"
        "h_ok_%=:\n"
        "v_exp_f32 v60, v60\n"
        "v_exp_f32 v61, v61\n"
        "v_exp_f32 v62, v62\n"
        "v_exp_f32 v63, v63\n"
        "v_cvt_pk_bf16_f32 v230, v60, v61\n"
        "v_cvt_pk_bf16_f32 v231, v62, v63\n"
        "v_mfma_f32_32x32x16_bf16 v[48:63], v[32:35], v[208:211], v[160:175]\n"
        "buffer_load_dwordx4 a[240:243], v28, s[12:15], 0 offen sc1\n"
        "v_exp_f32 v64, v64\n"
        "v_exp_f32 v65, v65\n"
        "v_exp_f32 v66, v66\n"
        "v_exp_f32 v67, v67\n"
        "v_cvt_pk_bf16_f32 v224, v64, v65\n"
        "v_cvt_pk_bf16_f32 v225, v66, v67\n"
        "v_mfma_f32_32x32x16_bf16 v[80:95], a[252:255], v[228:231], v[80:95]\n"
        "s_bfe_u32 s61, s20, 0x40000\n"
        "v_exp_f32 v68, v68\n"
        "v_exp_f32 v69, v69\n"
        "v_exp_f32 v70, v70\n"
        "v_exp_f32 v71, v71\n"
        "v_cvt_pk_bf16_f32 v226, v68, v69\n"
        "v_cvt_pk_bf16_f32 v227, v70, v71\n"
        "v_mfma_f32_32x32x16_bf16 v[48:63], v[36:39], v[212:215], v[48:63]\n"
        "buffer_load_dwordx4 a[244:247], v28, s[12:15], 0 offen offset:1024 sc1\n"
        "v_exp_f32 v72, v72\n"
        "v_exp_f32 v73, v73\n"
        "v_exp_f32 v74, v74\n"
        "v_exp_f32 v75, v75\n"
        "v_cvt_pk_bf16_f32 v228, v72, v73\n"
        "v_cvt_pk_bf16_f32 v229, v74, v75\n"
        "v_mfma_f32_32x32x16_bf16 v[96:111], v[40:43], v[224:227], v[96:111]\n"
        "s_lshl_b32 s61, s61, 12\n"
        "v_exp_f32 v76, v76\n"
        "v_exp_f32 v77, v77\n"
        "v_exp_f32 v78, v78\n"
        "v_exp_f32 v79, v79\n"
        "v_cvt_pk_bf16_f32 v230, v76, v77\n"
        "v_cvt_pk_bf16_f32 v231, v78, v79\n"
        "v_mfma_f32_32x32x16_bf16 v[64:79], v[32:35], v[216:219], v[176:191]\n"
        "buffer_load_dwordx4 a[248:251], v29, s[12:15], 0 offen sc1\n"
        "v_exp_f32 v48, v48\n"
        "v_exp_f32 v49, v49\n"
        "v_exp_f32 v50, v50\n"
        "v_exp_f32 v51, v51\n"
        "v_cvt_pk_bf16_f32 v224, v48, v49\n"
        "v_cvt_pk_bf16_f32 v225, v50, v51\n"
        "v_mfma_f32_32x32x16_bf16 v[96:111], v[44:47], v[228:231], v[96:111]\n"
        "s_add_u32 s40, s18, s61\n"
        "s_addc_u32 s41, s19, 0\n"
        "v_exp_f32 v52, v52\n"
        "v_exp_f32 v53, v53\n"
        "v_exp_f32 v54, v54\n"
        "v_exp_f32 v55, v55\n"
        "v_cvt_pk_bf16_f32 v226, v52, v53\n"
        "v_cvt_pk_bf16_f32 v227, v54, v55\n"
        "v_mfma_f32_32x32x16_bf16 v[64:79], v[36:39], v[220:223], v[64:79]\n"
        "buffer_load_dwordx4 a[252:255], v29, s[12:15], 0 offen offset:1024 sc1\n"
        "v_exp_f32 v56, v56\n"
        "v_exp_f32 v57, v57\n"
        "v_exp_f32 v58, v58\n"
        "v_exp_f32 v59, v59\n"
        "v_cvt_pk_bf16_f32 v228, v56, v57\n"
        "v_cvt_pk_bf16_f32 v229, v58, v59\n"
        "v_mfma_f32_32x32x16_bf16 v[112:127], v[40:43], v[224:227], v[112:127]\n"
        "buffer_load_dwordx4 a[0:3], v30, s[40:43], 0 offen sc1\n"
        "v_exp_f32 v60, v60\n"
        "v_exp_f32 v61, v61\n"
        "v_exp_f32 v62, v62\n"
        "v_exp_f32 v63, v63\n"
        "v_cvt_pk_bf16_f32 v230, v60, v61\n"
        "v_cvt_pk_bf16_f32 v231, v62, v63\n"
        "v_mfma_f32_32x32x16_bf16 v[48:63], v[32:35], v[192:195], v[128:143]\n"
        "buffer_load_dwordx4 a[4:7], v31, s[40:43], 0 offen sc1\n"
        "v_exp_f32 v64, v64\n"
        "v_exp_f32 v65, v65\n"
        "v_exp_f32 v66, v66\n"
        "v_exp_f32 v67, v67\n"
        "v_cvt_pk_bf16_f32 v224, v64, v65\n"
        "v_cvt_pk_bf16_f32 v225, v66, v67\n"
        "v_mfma_f32_32x32x16_bf16 v[112:127], v[44:47], v[228:231], v[112:127]\n"
        "buffer_load_dwordx4 a[8:11], v30, s[40:43], 0 offen offset:1024 sc1\n"
        "v_exp_f32 v68, v68\n"
        "v_exp_f32 v69, v69\n"
        "v_exp_f32 v70, v70\n"
        "v_exp_f32 v71, v71\n"
        "v_cvt_pk_bf16_f32 v226, v68, v69\n"
        "v_cvt_pk_bf16_f32 v227, v70, v71\n"
        "v_mfma_f32_32x32x16_bf16 v[48:63], v[36:39], v[196:199], v[48:63]\n"
        "buffer_load_dwordx4 a[12:15], v31, s[40:43], 0 offen offset:1024 sc1\n"
        "v_exp_f32 v72, v72\n"
        "v_exp_f32 v73, v73\n"
        "v_exp_f32 v74, v74\n"
        "v_exp_f32 v75, v75\n"
        "v_cvt_pk_bf16_f32 v228, v72, v73\n"
        "v_cvt_pk_bf16_f32 v229, v74, v75\n"
        "v_mfma_f32_32x32x16_bf16 v[232:247], v[40:43], v[224:227], v[232:247]\n"
        "s_waitcnt vmcnt(8)\n"
        "v_exp_f32 v76, v76\n"
        "v_exp_f32 v77, v77\n"
        "v_exp_f32 v78, v78\n"
        "v_exp_f32 v79, v79\n"
        "v_cvt_pk_bf16_f32 v230, v76, v77\n"
        "v_cvt_pk_bf16_f32 v231, v78, v79\n"
        "v_mfma_f32_32x32x16_bf16 v[64:79], a[224:227], v[200:203], v[144:159]\n"
        "s_add_u32 s62, s62, 0x10000\n"
        "v_exp_f32 v48, v48\n"
        "v_exp_f32 v49, v49\n"
        "v_exp_f32 v50, v50\n"
        "v_exp_f32 v51, v51\n"
        "v_cvt_pk_bf16_f32 v224, v48, v49\n"
        "v_cvt_pk_bf16_f32 v225, v50, v51\n"
        "v_mfma_f32_32x32x16_bf16 v[232:247], v[44:47], v[228:231], v[232:247]\n"
        "s_and_b32 s62, s62, 0xfffff\n"
        "v_exp_f32 v52, v52\n"
        "v_exp_f32 v53, v53\n"
        "v_exp_f32 v54, v54\n"
        "v_exp_f32 v55, v55\n"
        "v_cvt_pk_bf16_f32 v226, v52, v53\n"
        "v_cvt_pk_bf16_f32 v227, v54, v55\n"
        "v_mfma_f32_32x32x16_bf16 v[64:79], a[228:231], v[204:207], v[64:79]\n"
        "s_or_b32 s12, s95, s62\n"
        "v_exp_f32 v56, v56\n"
        "v_exp_f32 v57, v57\n"
        "v_exp_f32 v58, v58\n"
        "v_exp_f32 v59, v59\n"
        "v_cvt_pk_bf16_f32 v228, v56, v57\n"
        "v_cvt_pk_bf16_f32 v229, v58, v59\n"
        "v_mfma_f32_32x32x16_bf16 v[80:95], v[40:43], v[224:227], v[80:95]\n"
        "buffer_load_dwordx4 a[16:19], v30, s[40:43], 0 offen offset:2048 sc1\n"
        "v_exp_f32 v60, v60\n"
        "v_exp_f32 v61, v61\n"
        "v_exp_f32 v62, v62\n"
        "v_exp_f32 v63, v63\n"
        "v_cvt_pk_bf16_f32 v230, v60, v61\n"
        "v_cvt_pk_bf16_f32 v231, v62, v63\n"
        "v_mfma_f32_32x32x16_bf16 v[48:63], a[224:227], v[208:211], v[160:175]\n"
        "buffer_load_dwordx4 v[32:35], v28, s[12:15], 0 offen sc1\n"
        "v_exp_f32 v64, v64\n"
        "v_exp_f32 v65, v65\n"
        "v_exp_f32 v66, v66\n"
        "v_exp_f32 v67, v67\n"
        "v_cvt_pk_bf16_f32 v224, v64, v65\n"
        "v_cvt_pk_bf16_f32 v225, v66, v67\n"
        "v_mfma_f32_32x32x16_bf16 v[80:95], v[44:47], v[228:231], v[80:95]\n"
        "buffer_load_dwordx4 a[20:23], v31, s[40:43], 0 offen offset:2048 sc1\n"
        "v_exp_f32 v68, v68\n"
        "v_exp_f32 v69, v69\n"
        "v_exp_f32 v70, v70\n"
        "v_exp_f32 v71, v71\n"
        "v_cvt_pk_bf16_f32 v226, v68, v69\n"
        "v_cvt_pk_bf16_f32 v227, v70, v71\n"
        "v_mfma_f32_32x32x16_bf16 v[48:63], a[228:231], v[212:215], v[48:63]\n"
        "buffer_load_dwordx4 v[36:39], v28, s[12:15], 0 offen offset:1024 sc1\n"
        "v_exp_f32 v72, v72\n"
        "v_exp_f32 v73, v73\n"
        "v_exp_f32 v74, v74\n"
        "v_exp_f32 v75, v75\n"
        "v_cvt_pk_bf16_f32 v228, v72, v73\n"
        "v_cvt_pk_bf16_f32 v229, v74, v75\n"
        "v_mfma_f32_32x32x16_bf16 v[96:111], a[232:235], v[224:227], v[96:111]\n"
        "buffer_load_dwordx4 a[24:27], v30, s[40:43], 0 offen offset:3072 sc1\n"
        "v_exp_f32 v76, v76\n"
        "v_exp_f32 v77, v77\n"
        "v_exp_f32 v78, v78\n"
        "v_exp_f32 v79, v79\n"
        "v_cvt_pk_bf16_f32 v230, v76, v77\n"
        "v_cvt_pk_bf16_f32 v231, v78, v79\n"
        "v_mfma_f32_32x32x16_bf16 v[64:79], a[224:227], v[216:219], v[176:191]\n"
        "buffer_load_dwordx4 v[40:43], v29, s[12:15], 0 offen sc1\n"
        "v_exp_f32 v48, v48\n"
        "v_exp_f32 v49, v49\n"
        "v_exp_f32 v50, v50\n"
        "v_exp_f32 v51, v51\n"
        "v_cvt_pk_bf16_f32 v224, v48, v49\n"
        "v_cvt_pk_bf16_f32 v225, v50, v51\n"
        "v_mfma_f32_32x32x16_bf16 v[96:111], a[236:239], v[228:231], v[96:111]\n"
        "buffer_load_dwordx4 a[28:31], v31, s[40:43], 0 offen offset:3072 sc1\n"
        "v_exp_f32 v52, v52\n"
        "v_exp_f32 v53, v53\n"
        "v_exp_f32 v54, v54\n"
        "v_exp_f32 v55, v55\n"
        "v_cvt_pk_bf16_f32 v226, v52, v53\n"
        "v_cvt_pk_bf16_f32 v227, v54, v55\n"
        "v_mfma_f32_32x32x16_bf16 v[64:79], a[228:231], v[220:223], v[64:79]\n"
        "buffer_load_dwordx4 v[44:47], v29, s[12:15], 0 offen offset:1024 sc1\n"
        "v_exp_f32 v56, v56\n"
        "v_exp_f32 v57, v57\n"
        "v_exp_f32 v58, v58\n"
        "v_exp_f32 v59, v59\n"
        "v_cvt_pk_bf16_f32 v228, v56, v57\n"
        "v_cvt_pk_bf16_f32 v229, v58, v59\n"
        "v_mfma_f32_32x32x16_bf16 v[112:127], a[232:235], v[224:227], v[112:127]\n"
        "s_bfe_u32 s61, s20, 0x40004\n"
        "v_exp_f32 v60, v60\n"
        "v_exp_f32 v61, v61\n"
        "v_exp_f32 v62, v62\n"
        "v_exp_f32 v63, v63\n"
        "v_cvt_pk_bf16_f32 v230, v60, v61\n"
        "v_cvt_pk_bf16_f32 v231, v62, v63\n"
        "v_mfma_f32_32x32x16_bf16 v[48:63], a[224:227], v[192:195], v[128:143]\n"
        "s_lshl_b32 s61, s61, 12\n"
        "v_exp_f32 v64, v64\n"
        "v_exp_f32 v65, v65\n"
        "v_exp_f32 v66, v66\n"
        "v_exp_f32 v67, v67\n"
        "v_cvt_pk_bf16_f32 v224, v64, v65\n"
        "v_cvt_pk_bf16_f32 v225, v66, v67\n"
        "v_mfma_f32_32x32x16_bf16 v[112:127], a[236:239], v[228:231], v[112:127]\n"
        "s_add_u32 s40, s18, s61\n"
        "s_addc_u32 s41, s19, 0\n"
        "v_exp_f32 v68, v68\n"
        "v_exp_f32 v69, v69\n"
        "v_exp_f32 v70, v70\n"
        "v_exp_f32 v71, v71\n"
        "v_cvt_pk_bf16_f32 v226, v68, v69\n"
        "v_cvt_pk_bf16_f32 v227, v70, v71\n"
        "v_mfma_f32_32x32x16_bf16 v[48:63], a[228:231], v[196:199], v[48:63]\n"
        "buffer_load_dwordx4 a[32:35], v30, s[40:43], 0 offen sc1\n"
        "v_exp_f32 v72, v72\n"
        "v_exp_f32 v73, v73\n"
        "v_exp_f32 v74, v74\n"
        "v_exp_f32 v75, v75\n"
        "v_cvt_pk_bf16_f32 v228, v72, v73\n"
        "v_cvt_pk_bf16_f32 v229, v74, v75\n"
        "v_mfma_f32_32x32x16_bf16 v[232:247], a[232:235], v[224:227], v[232:247]\n"
        "s_waitcnt vmcnt(13)\n"
        "v_exp_f32 v76, v76\n"
        "v_exp_f32 v77, v77\n"
        "v_exp_f32 v78, v78\n"
        "v_exp_f32 v79, v79\n"
        "v_cvt_pk_bf16_f32 v230, v76, v77\n"
        "v_cvt_pk_bf16_f32 v231, v78, v79\n"
        "v_mfma_f32_32x32x16_bf16 v[64:79], a[240:243], v[200:203], v[144:159]\n"
        "s_add_u32 s62, s62, 0x10000\n"
        "v_exp_f32 v48, v48\n"
        "v_exp_f32 v49, v49\n"
        "v_exp_f32 v50, v50\n"
        "v_exp_f32 v51, v51\n"
        "v_cvt_pk_bf16_f32 v224, v48, v49\n"
        "v_cvt_pk_bf16_f32 v225, v50, v51\n"
        "v_mfma_f32_32x32x16_bf16 v[232:247], a[236:239], v[228:231], v[232:247]\n"
        "s_and_b32 s62, s62, 0xfffff\n"
        "v_exp_f32 v52, v52\n"
        "v_exp_f32 v53, v53\n"
        "v_exp_f32 v54, v54\n"
        "v_exp_f32 v55, v55\n"
        "v_cvt_pk_bf16_f32 v226, v52, v53\n"
        "v_cvt_pk_bf16_f32 v227, v54, v55\n"
        "v_mfma_f32_32x32x16_bf16 v[64:79], a[244:247], v[204:207], v[64:79]\n"
        "s_or_b32 s12, s95, s62\n"
        "v_exp_f32 v56, v56\n"
        "v_exp_f32 v57, v57\n"
        "v_exp_f32 v58, v58\n"
        "v_exp_f32 v59, v59\n"
        "v_cvt_pk_bf16_f32 v228, v56, v57\n"
        "v_cvt_pk_bf16_f32 v229, v58, v59\n"
        "v_mfma_f32_32x32x16_bf16 v[80:95], a[232:235], v[224:227], v[80:95]\n"
        "buffer_load_dwordx4 a[36:39], v31, s[40:43], 0 offen sc1\n"
        "v_exp_f32 v60, v60\n"
        "v_exp_f32 v61, v61\n"
        "v_exp_f32 v62, v62\n"
        "v_exp_f32 v63, v63\n"
        "v_cvt_pk_bf16_f32 v230, v60, v61\n"
        "v_cvt_pk_bf16_f32 v231, v62, v63\n"
        "v_mfma_f32_32x32x16_bf16 v[48:63], a[240:243], v[208:211], v[160:175]\n"
        "buffer_load_dwordx4 a[224:227], v28, s[12:15], 0 offen sc1\n"
        "v_exp_f32 v64, v64\n"
        "v_exp_f32 v65, v65\n"
        "v_exp_f32 v66, v66\n"
        "v_exp_f32 v67, v67\n"
        "v_cvt_pk_bf16_f32 v224, v64, v65\n"
        "v_cvt_pk_bf16_f32 v225, v66, v67\n"
        "v_mfma_f32_32x32x16_bf16 v[80:95], a[236:239], v[228:231], v[80:95]\n"
        "buffer_load_dwordx4 a[40:43], v30, s[40:43], 0 offen offset:1024 sc1\n"
        "v_exp_f32 v68, v68\n"
        "v_exp_f32 v69, v69\n"
        "v_exp_f32 v70, v70\n"
        "v_exp_f32 v71, v71\n"
        "v_cvt_pk_bf16_f32 v226, v68, v69\n"
        "v_cvt_pk_bf16_f32 v227, v70, v71\n"
        "v_mfma_f32_32x32x16_bf16 v[48:63], a[244:247], v[212:215], v[48:63]\n"
        "buffer_load_dwordx4 a[228:231], v28, s[12:15], 0 offen offset:1024 sc1\n"
        "v_exp_f32 v72, v72\n"
        "v_exp_f32 v73, v73\n"
        "v_exp_f32 v74, v74\n"
        "v_exp_f32 v75, v75\n"
        "v_cvt_pk_bf16_f32 v228, v72, v73\n"
        "v_cvt_pk_bf16_f32 v229, v74, v75\n"
        "v_mfma_f32_32x32x16_bf16 v[96:111], a[248:251], v[224:227], v[96:111]\n"
        "buffer_load_dwordx4 a[44:47], v31, s[40:43], 0 offen offset:1024 sc1\n"
        "v_exp_f32 v76, v76\n"
        "v_exp_f32 v77, v77\n"
        "v_exp_f32 v78, v78\n"
        "v_exp_f32 v79, v79\n"
        "v_cvt_pk_bf16_f32 v230, v76, v77\n"
        "v_cvt_pk_bf16_f32 v231, v78, v79\n"
        "v_mfma_f32_32x32x16_bf16 v[64:79], a[240:243], v[216:219], v[176:191]\n"
        "buffer_load_dwordx4 a[232:235], v29, s[12:15], 0 offen sc1\n"
        "v_exp_f32 v48, v48\n"
        "v_exp_f32 v49, v49\n"
        "v_exp_f32 v50, v50\n"
        "v_exp_f32 v51, v51\n"
        "v_cvt_pk_bf16_f32 v224, v48, v49\n"
        "v_cvt_pk_bf16_f32 v225, v50, v51\n"
        "v_mfma_f32_32x32x16_bf16 v[96:111], a[252:255], v[228:231], v[96:111]\n"
        "buffer_load_dwordx4 a[48:51], v30, s[40:43], 0 offen offset:2048 sc1\n"
        "v_exp_f32 v52, v52\n"
        "v_exp_f32 v53, v53\n"
        "v_exp_f32 v54, v54\n"
        "v_exp_f32 v55, v55\n"
        "v_cvt_pk_bf16_f32 v226, v52, v53\n"
        "v_cvt_pk_bf16_f32 v227, v54, v55\n"
        "v_mfma_f32_32x32x16_bf16 v[64:79], a[244:247], v[220:223], v[64:79]\n"
        "buffer_load_dwordx4 a[236:239], v29, s[12:15], 0 offen offset:1024 sc1\n"
        "v_exp_f32 v56, v56\n"
        "v_exp_f32 v57, v57\n"
        "v_exp_f32 v58, v58\n"
        "v_exp_f32 v59, v59\n"
        "v_cvt_pk_bf16_f32 v228, v56, v57\n"
        "v_cvt_pk_bf16_f32 v229, v58, v59\n"
        "v_mfma_f32_32x32x16_bf16 v[112:127], a[248:251], v[224:227], v[112:127]\n"
        "buffer_load_dwordx4 a[52:55], v31, s[40:43], 0 offen offset:2048 sc1\n"
        "v_exp_f32 v60, v60\n"
        "v_exp_f32 v61, v61\n"
        "v_exp_f32 v62, v62\n"
        "v_exp_f32 v63, v63\n"
        "v_cvt_pk_bf16_f32 v230, v60, v61\n"
        "v_cvt_pk_bf16_f32 v231, v62, v63\n"
        "v_mfma_f32_32x32x16_bf16 v[48:63], a[240:243], v[192:195], v[128:143]\n"
        "buffer_load_dwordx4 a[56:59], v30, s[40:43], 0 offen offset:3072 sc1\n"
        "v_exp_f32 v64, v64\n"
        "v_exp_f32 v65, v65\n"
        "v_exp_f32 v66, v66\n"
        "v_exp_f32 v67, v67\n"
        "v_cvt_pk_bf16_f32 v224, v64, v65\n"
        "v_cvt_pk_bf16_f32 v225, v66, v67\n"
        "v_mfma_f32_32x32x16_bf16 v[112:127], a[252:255], v[228:231], v[112:127]\n"
        "buffer_load_dwordx4 a[60:63], v31, s[40:43], 0 offen offset:3072 sc1\n"
        "v_exp_f32 v68, v68\n"
        "v_exp_f32 v69, v69\n"
        "v_exp_f32 v70, v70\n"
        "v_exp_f32 v71, v71\n"
        "v_cvt_pk_bf16_f32 v226, v68, v69\n"
        "v_cvt_pk_bf16_f32 v227, v70, v71\n"
        "v_mfma_f32_32x32x16_bf16 v[48:63], a[244:247], v[196:199], v[48:63]\n"
        "s_bfe_u32 s61, s20, 0x40008\n"
        "v_exp_f32 v72, v72\n"
        "v_exp_f32 v73, v73\n"
        "v_exp_f32 v74, v74\n"
        "v_exp_f32 v75, v75\n"
        "v_cvt_pk_bf16_f32 v228, v72, v73\n"
        "v_cvt_pk_bf16_f32 v229, v74, v75\n"
        "v_mfma_f32_32x32x16_bf16 v[232:247], a[248:251], v[224:227], v[232:247]\n"
        "s_waitcnt vmcnt(12)\n"
        "v_exp_f32 v76, v76\n"
        "v_exp_f32 v77, v77\n"
        "v_exp_f32 v78, v78\n"
        "v_exp_f32 v79, v79\n"
        "v_cvt_pk_bf16_f32 v230, v76, v77\n"
        "v_cvt_pk_bf16_f32 v231, v78, v79\n"
        "v_mfma_f32_32x32x16_bf16 v[64:79], v[32:35], v[200:203], v[144:159]\n"
        "s_add_u32 s62, s62, 0x10000\n"
        "v_exp_f32 v48, v48\n"
        "v_exp_f32 v49, v49\n"
        "v_exp_f32 v50, v50\n"
        "v_exp_f32 v51, v51\n"
        "v_cvt_pk_bf16_f32 v224, v48, v49\n"
        "v_cvt_pk_bf16_f32 v225, v50, v51\n"
        "v_mfma_f32_32x32x16_bf16 v[232:247], a[252:255], v[228:231], v[232:247]\n"
        "s_and_b32 s62, s62, 0xfffff\n"
        "v_exp_f32 v52, v52\n"
        "v_exp_f32 v53, v53\n"
        "v_exp_f32 v54, v54\n"
        "v_exp_f32 v55, v55\n"
        "v_cvt_pk_bf16_f32 v226, v52, v53\n"
        "v_cvt_pk_bf16_f32 v227, v54, v55\n"
        "v_mfma_f32_32x32x16_bf16 v[64:79], v[36:39], v[204:207], v[64:79]\n"
        "s_or_b32 s12, s95, s62\n"
        "v_exp_f32 v56, v56\n"
        "v_exp_f32 v57, v57\n"
        "v_exp_f32 v58, v58\n"
        "v_exp_f32 v59, v59\n"
        "v_cvt_pk_bf16_f32 v228, v56, v57\n"
        "v_cvt_pk_bf16_f32 v229, v58, v59\n"
        "v_mfma_f32_32x32x16_bf16 v[80:95], a[248:251], v[224:227], v[80:95]\n"
        "s_lshl_b32 s61, s61, 12\n"
        "v_exp_f32 v60, v60\n"
        "v_exp_f32 v61, v61\n"
        "v_exp_f32 v62, v62\n"
        "v_exp_f32 v63, v63\n"
        "v_cvt_pk_bf16_f32 v230, v60, v61\n"
        "v_cvt_pk_bf16_f32 v231, v62, v63\n"
        "v_mfma_f32_32x32x16_bf16 v[48:63], v[32:35], v[208:211], v[160:175]\n"
        "buffer_load_dwordx4 a[240:243], v28, s[12:15], 0 offen sc1\n"
        "v_exp_f32 v64, v64\n"
        "v_exp_f32 v65, v65\n"
        "v_exp_f32 v66, v66\n"
        "v_exp_f32 v67, v67\n"
        "v_cvt_pk_bf16_f32 v224, v64, v65\n"
        "v_cvt_pk_bf16_f32 v225, v66, v67\n"
        "v_mfma_f32_32x32x16_bf16 v[80:95], a[252:255], v[228:231], v[80:95]\n"
        "s_add_u32 s40, s18, s61\n"
        "s_addc_u32 s41, s19, 0\n"
        "v_exp_f32 v68, v68\n"
        "v_exp_f32 v69, v69\n"
        "v_exp_f32 v70, v70\n"
        "v_exp_f32 v71, v71\n"
        "v_cvt_pk_bf16_f32 v226, v68, v69\n"
        "v_cvt_pk_bf16_f32 v227, v70, v71\n"
        "v_mfma_f32_32x32x16_bf16 v[48:63], v[36:39], v[212:215], v[48:63]\n"
        "buffer_load_dwordx4 a[244:247], v28, s[12:15], 0 offen offset:1024 sc1\n"
        "v_exp_f32 v72, v72\n"
        "v_exp_f32 v73, v73\n"
        "v_exp_f32 v74, v74\n"
        "v_exp_f32 v75, v75\n"
        "v_cvt_pk_bf16_f32 v228, v72, v73\n"
        "v_cvt_pk_bf16_f32 v229, v74, v75\n"
        "v_mfma_f32_32x32x16_bf16 v[96:111], v[40:43], v[224:227], v[96:111]\n"
        "buffer_load_dwordx4 a[64:67], v30, s[40:43], 0 offen sc1\n"
        "v_exp_f32 v76, v76\n"
        "v_exp_f32 v77, v77\n"
        "v_exp_f32 v78, v78\n"
        "v_exp_f32 v79, v79\n"
        "v_cvt_pk_bf16_f32 v230, v76, v77\n"
        "v_cvt_pk_bf16_f32 v231, v78, v79\n"
        "v_mfma_f32_32x32x16_bf16 v[64:79], v[32:35], v[216:219], v[176:191]\n"
        "buffer_load_dwordx4 a[248:251], v29, s[12:15], 0 offen sc1\n"
        "v_exp_f32 v48, v48\n"
        "v_exp_f32 v49, v49\n"
        "v_exp_f32 v50, v50\n"
        "v_exp_f32 v51, v51\n"
        "v_cvt_pk_bf16_f32 v224, v48, v49\n"
        "v_cvt_pk_bf16_f32 v225, v50, v51\n"
        "v_mfma_f32_32x32x16_bf16 v[96:111], v[44:47], v[228:231], v[96:111]\n"
        "buffer_load_dwordx4 a[68:71], v31, s[40:43], 0 offen sc1\n"
        "v_exp_f32 v52, v52\n"
        "v_exp_f32 v53, v53\n"
        "v_exp_f32 v54, v54\n"
        "v_exp_f32 v55, v55\n"
        "v_cvt_pk_bf16_f32 v226, v52, v53\n"
        "v_cvt_pk_bf16_f32 v227, v54, v55\n"
        "v_mfma_f32_32x32x16_bf16 v[64:79], v[36:39], v[220:223], v[64:79]\n"
        "buffer_load_dwordx4 a[252:255], v29, s[12:15], 0 offen offset:1024 sc1\n"
        "v_exp_f32 v56, v56\n"
        "v_exp_f32 v57, v57\n"
        "v_exp_f32 v58, v58\n"
        "v_exp_f32 v59, v59\n"
        "v_cvt_pk_bf16_f32 v228, v56, v57\n"
        "v_cvt_pk_bf16_f32 v229, v58, v59\n"
        "v_mfma_f32_32x32x16_bf16 v[112:127], v[40:43], v[224:227], v[112:127]\n"
        "buffer_load_dwordx4 a[72:75], v30, s[40:43], 0 offen offset:1024 sc1\n"
        "v_exp_f32 v60, v60\n"
        "v_exp_f32 v61, v61\n"
        "v_exp_f32 v62, v62\n"
        "v_exp_f32 v63, v63\n"
        "v_cvt_pk_bf16_f32 v230, v60, v61\n"
        "v_cvt_pk_bf16_f32 v231, v62, v63\n"
        "v_mfma_f32_32x32x16_bf16 v[48:63], v[32:35], v[192:195], v[128:143]\n"
        "buffer_load_dwordx4 a[76:79], v31, s[40:43], 0 offen offset:1024 sc1\n"
        "v_exp_f32 v64, v64\n"
        "v_exp_f32 v65, v65\n"
        "v_exp_f32 v66, v66\n"
        "v_exp_f32 v67, v67\n"
        "v_cvt_pk_bf16_f32 v224, v64, v65\n"
        "v_cvt_pk_bf16_f32 v225, v66, v67\n"
        "v_mfma_f32_32x32x16_bf16 v[112:127], v[44:47], v[228:231], v[112:127]\n"
        "buffer_load_dwordx4 a[80:83], v30, s[40:43], 0 offen offset:2048 sc1\n"
        "v_exp_f32 v68, v68\n"
        "v_exp_f32 v69, v69\n"
        "v_exp_f32 v70, v70\n"
        "v_exp_f32 v71, v71\n"
        "v_cvt_pk_bf16_f32 v226, v68, v69\n"
        "v_cvt_pk_bf16_f32 v227, v70, v71\n"
        "v_mfma_f32_32x32x16_bf16 v[48:63], v[36:39], v[196:199], v[48:63]\n"
        "buffer_load_dwordx4 a[84:87], v31, s[40:43], 0 offen offset:2048 sc1\n"
        "v_exp_f32 v72, v72\n"
        "v_exp_f32 v73, v73\n"
        "v_exp_f32 v74, v74\n"
        "v_exp_f32 v75, v75\n"
        "v_cvt_pk_bf16_f32 v228, v72, v73\n"
        "v_cvt_pk_bf16_f32 v229, v74, v75\n"
        "v_mfma_f32_32x32x16_bf16 v[232:247], v[40:43], v[224:227], v[232:247]\n"
        "s_waitcnt vmcnt(13)\n"
        "v_exp_f32 v76, v76\n"
        "v_exp_f32 v77, v77\n"
        "v_exp_f32 v78, v78\n"
        "v_exp_f32 v79, v79\n"
        "v_cvt_pk_bf16_f32 v230, v76, v77\n"
        "v_cvt_pk_bf16_f32 v231, v78, v79\n"
        "v_mfma_f32_32x32x16_bf16 v[64:79], a[224:227], v[200:203], v[144:159]\n"
        "s_add_u32 s62, s62, 0x10000\n"
        "v_exp_f32 v48, v48\n"
        "v_exp_f32 v49, v49\n"
        "v_exp_f32 v50, v50\n"
        "v_exp_f32 v51, v51\n"
        "v_cvt_pk_bf16_f32 v224, v48, v49\n"
        "v_cvt_pk_bf16_f32 v225, v50, v51\n"
        "v_mfma_f32_32x32x16_bf16 v[232:247], v[44:47], v[228:231], v[232:247]\n"
        "s_and_b32 s62, s62, 0xfffff\n"
        "v_exp_f32 v52, v52\n"
        "v_exp_f32 v53, v53\n"
        "v_exp_f32 v54, v54\n"
        "v_exp_f32 v55, v55\n"
        "v_cvt_pk_bf16_f32 v226, v52, v53\n"
        "v_cvt_pk_bf16_f32 v227, v54, v55\n"
        "v_mfma_f32_32x32x16_bf16 v[64:79], a[228:231], v[204:207], v[64:79]\n"
        "s_or_b32 s12, s95, s62\n"
        "v_exp_f32 v56, v56\n"
        "v_exp_f32 v57, v57\n"
        "v_exp_f32 v58, v58\n"
        "v_exp_f32 v59, v59\n"
        "v_cvt_pk_bf16_f32 v228, v56, v57\n"
        "v_cvt_pk_bf16_f32 v229, v58, v59\n"
        "v_mfma_f32_32x32x16_bf16 v[80:95], v[40:43], v[224:227], v[80:95]\n"
        "buffer_load_dwordx4 a[88:91], v30, s[40:43], 0 offen offset:3072 sc1\n"
        "v_exp_f32 v60, v60\n"
        "v_exp_f32 v61, v61\n"
        "v_exp_f32 v62, v62\n"
        "v_exp_f32 v63, v63\n"
        "v_cvt_pk_bf16_f32 v230, v60, v61\n"
        "v_cvt_pk_bf16_f32 v231, v62, v63\n"
        "v_mfma_f32_32x32x16_bf16 v[48:63], a[224:227], v[208:211], v[160:175]\n"
        "buffer_load_dwordx4 v[32:35], v28, s[12:15], 0 offen sc1\n"
        "v_exp_f32 v64, v64\n"
        "v_exp_f32 v65, v65\n"
        "v_exp_f32 v66, v66\n"
        "v_exp_f32 v67, v67\n"
        "v_cvt_pk_bf16_f32 v224, v64, v65\n"
        "v_cvt_pk_bf16_f32 v225, v66, v67\n"
        "v_mfma_f32_32x32x16_bf16 v[80:95], v[44:47], v[228:231], v[80:95]\n"
        "buffer_load_dwordx4 a[92:95], v31, s[40:43], 0 offen offset:3072 sc1\n"
        "v_exp_f32 v68, v68\n"
        "v_exp_f32 v69, v69\n"
        "v_exp_f32 v70, v70\n"
        "v_exp_f32 v71, v71\n"
        "v_cvt_pk_bf16_f32 v226, v68, v69\n"
        "v_cvt_pk_bf16_f32 v227, v70, v71\n"
        "v_mfma_f32_32x32x16_bf16 v[48:63], a[228:231], v[212:215], v[48:63]\n"
        "buffer_load_dwordx4 v[36:39], v28, s[12:15], 0 offen offset:1024 sc1\n"
        "v_exp_f32 v72, v72\n"
        "v_exp_f32 v73, v73\n"
        "v_exp_f32 v74, v74\n"
        "v_exp_f32 v75, v75\n"
        "v_cvt_pk_bf16_f32 v228, v72, v73\n"
        "v_cvt_pk_bf16_f32 v229, v74, v75\n"
        "v_mfma_f32_32x32x16_bf16 v[96:111], a[232:235], v[224:227], v[96:111]\n"
        "s_bfe_u32 s61, s20, 0x4000c\n"
        "v_exp_f32 v76, v76\n"
        "v_exp_f32 v77, v77\n"
        "v_exp_f32 v78, v78\n"
        "v_exp_f32 v79, v79\n"
        "v_cvt_pk_bf16_f32 v230, v76, v77\n"
        "v_cvt_pk_bf16_f32 v231, v78, v79\n"
        "v_mfma_f32_32x32x16_bf16 v[64:79], a[224:227], v[216:219], v[176:191]\n"
        "buffer_load_dwordx4 v[40:43], v29, s[12:15], 0 offen sc1\n"
        "v_exp_f32 v48, v48\n"
        "v_exp_f32 v49, v49\n"
        "v_exp_f32 v50, v50\n"
        "v_exp_f32 v51, v51\n"
        "v_cvt_pk_bf16_f32 v224, v48, v49\n"
        "v_cvt_pk_bf16_f32 v225, v50, v51\n"
        "v_mfma_f32_32x32x16_bf16 v[96:111], a[236:239], v[228:231], v[96:111]\n"
        "s_lshl_b32 s61, s61, 12\n"
        "v_exp_f32 v52, v52\n"
        "v_exp_f32 v53, v53\n"
        "v_exp_f32 v54, v54\n"
        "v_exp_f32 v55, v55\n"
        "v_cvt_pk_bf16_f32 v226, v52, v53\n"
        "v_cvt_pk_bf16_f32 v227, v54, v55\n"
        "v_mfma_f32_32x32x16_bf16 v[64:79], a[228:231], v[220:223], v[64:79]\n"
        "buffer_load_dwordx4 v[44:47], v29, s[12:15], 0 offen offset:1024 sc1\n"
        "v_exp_f32 v56, v56\n"
        "v_exp_f32 v57, v57\n"
        "v_exp_f32 v58, v58\n"
        "v_exp_f32 v59, v59\n"
        "v_cvt_pk_bf16_f32 v228, v56, v57\n"
        "v_cvt_pk_bf16_f32 v229, v58, v59\n"
        "v_mfma_f32_32x32x16_bf16 v[112:127], a[232:235], v[224:227], v[112:127]\n"
        "s_add_u32 s40, s18, s61\n"
        "s_addc_u32 s41, s19, 0\n"
        "v_exp_f32 v60, v60\n"
        "v_exp_f32 v61, v61\n"
        "v_exp_f32 v62, v62\n"
        "v_exp_f32 v63, v63\n"
        "v_cvt_pk_bf16_f32 v230, v60, v61\n"
        "v_cvt_pk_bf16_f32 v231, v62, v63\n"
        "v_mfma_f32_32x32x16_bf16 v[48:63], a[224:227], v[192:195], v[128:143]\n"
        "buffer_load_dwordx4 a[96:99], v30, s[40:43], 0 offen sc1\n"
        "v_exp_f32 v64, v64\n"
        "v_exp_f32 v65, v65\n"
        "v_exp_f32 v66, v66\n"
        "v_exp_f32 v67, v67\n"
        "v_cvt_pk_bf16_f32 v224, v64, v65\n"
        "v_cvt_pk_bf16_f32 v225, v66, v67\n"
        "v_mfma_f32_32x32x16_bf16 v[112:127], a[236:239], v[228:231], v[112:127]\n"
        "buffer_load_dwordx4 a[100:103], v31, s[40:43], 0 offen sc1\n"
        "v_exp_f32 v68, v68\n"
        "v_exp_f32 v69, v69\n"
        "v_exp_f32 v70, v70\n"
        "v_exp_f32 v71, v71\n"
        "v_cvt_pk_bf16_f32 v226, v68, v69\n"
        "v_cvt_pk_bf16_f32 v227, v70, v71\n"
        "v_mfma_f32_32x32x16_bf16 v[48:63], a[228:231], v[196:199], v[48:63]\n"
        "buffer_load_dwordx4 a[104:107], v30, s[40:43], 0 offen offset:1024 sc1\n"
        "v_exp_f32 v72, v72\n"
        "v_exp_f32 v73, v73\n"
        "v_exp_f32 v74, v74\n"
        "v_exp_f32 v75, v75\n"
        "v_cvt_pk_bf16_f32 v228, v72, v73\n"
        "v_cvt_pk_bf16_f32 v229, v74, v75\n"
        "v_mfma_f32_32x32x16_bf16 v[232:247], a[232:235], v[224:227], v[232:247]\n"
        "s_waitcnt vmcnt(13)\n"
        "v_exp_f32 v76, v76\n"
        "v_exp_f32 v77, v77\n"
        "v_exp_f32 v78, v78\n"
        "v_exp_f32 v79, v79\n"
        "v_cvt_pk_bf16_f32 v230, v76, v77\n"
        "v_cvt_pk_bf16_f32 v231, v78, v79\n"
        "v_mfma_f32_32x32x16_bf16 v[64:79], a[240:243], v[200:203], v[144:159]\n"
        "s_add_u32 s62, s62, 0x10000\n"
        "v_exp_f32 v48, v48\n"
        "v_exp_f32 v49, v49\n"
        "v_exp_f32 v50, v50\n"
        "v_exp_f32 v51, v51\n"
        "v_cvt_pk_bf16_f32 v224, v48, v49\n"
        "v_cvt_pk_bf16_f32 v225, v50, v51\n"
        "v_mfma_f32_32x32x16_bf16 v[232:247], a[236:239], v[228:231], v[232:247]\n"
        "s_and_b32 s62, s62, 0xfffff\n"
        "v_exp_f32 v52, v52\n"
        "v_exp_f32 v53, v53\n"
        "v_exp_f32 v54, v54\n"
        "v_exp_f32 v55, v55\n"
        "v_cvt_pk_bf16_f32 v226, v52, v53\n"
        "v_cvt_pk_bf16_f32 v227, v54, v55\n"
        "v_mfma_f32_32x32x16_bf16 v[64:79], a[244:247], v[204:207], v[64:79]\n"
        "s_or_b32 s12, s95, s62\n"
        "v_exp_f32 v56, v56\n"
        "v_exp_f32 v57, v57\n"
        "v_exp_f32 v58, v58\n"
        "v_exp_f32 v59, v59\n"
        "v_cvt_pk_bf16_f32 v228, v56, v57\n"
        "v_cvt_pk_bf16_f32 v229, v58, v59\n"
        "v_mfma_f32_32x32x16_bf16 v[80:95], a[232:235], v[224:227], v[80:95]\n"
        "buffer_load_dwordx4 a[108:111], v31, s[40:43], 0 offen offset:1024 sc1\n"
        "v_exp_f32 v60, v60\n"
        "v_exp_f32 v61, v61\n"
        "v_exp_f32 v62, v62\n"
        "v_exp_f32 v63, v63\n"
        "v_cvt_pk_bf16_f32 v230, v60, v61\n"
        "v_cvt_pk_bf16_f32 v231, v62, v63\n"
        "v_mfma_f32_32x32x16_bf16 v[48:63], a[240:243], v[208:211], v[160:175]\n"
        "buffer_load_dwordx4 a[224:227], v28, s[12:15], 0 offen sc1\n"
        "v_exp_f32 v64, v64\n"
        "v_exp_f32 v65, v65\n"
        "v_exp_f32 v66, v66\n"
        "v_exp_f32 v67, v67\n"
        "v_cvt_pk_bf16_f32 v224, v64, v65\n"
        "v_cvt_pk_bf16_f32 v225, v66, v67\n"
        "v_mfma_f32_32x32x16_bf16 v[80:95], a[236:239], v[228:231], v[80:95]\n"
        "buffer_load_dwordx4 a[112:115], v30, s[40:43], 0 offen offset:2048 sc1\n"
        "v_exp_f32 v68, v68\n"
        "v_exp_f32 v69, v69\n"
        "v_exp_f32 v70, v70\n"
        "v_exp_f32 v71, v71\n"
        "v_cvt_pk_bf16_f32 v226, v68, v69\n"
        "v_cvt_pk_bf16_f32 v227, v70, v71\n"
        "v_mfma_f32_32x32x16_bf16 v[48:63], a[244:247], v[212:215], v[48:63]\n"
        "buffer_load_dwordx4 a[228:231], v28, s[12:15], 0 offen offset:1024 sc1\n"
        "v_exp_f32 v72, v72\n"
        "v_exp_f32 v73, v73\n"
        "v_exp_f32 v74, v74\n"
        "v_exp_f32 v75, v75\n"
        "v_cvt_pk_bf16_f32 v228, v72, v73\n"
        "v_cvt_pk_bf16_f32 v229, v74, v75\n"
        "v_mfma_f32_32x32x16_bf16 v[96:111], a[248:251], v[224:227], v[96:111]\n"
        "buffer_load_dwordx4 a[116:119], v31, s[40:43], 0 offen offset:2048 sc1\n"
        "v_exp_f32 v76, v76\n"
        "v_exp_f32 v77, v77\n"
        "v_exp_f32 v78, v78\n"
        "v_exp_f32 v79, v79\n"
        "v_cvt_pk_bf16_f32 v230, v76, v77\n"
        "v_cvt_pk_bf16_f32 v231, v78, v79\n"
        "v_mfma_f32_32x32x16_bf16 v[64:79], a[240:243], v[216:219], v[176:191]\n"
        "buffer_load_dwordx4 a[232:235], v29, s[12:15], 0 offen sc1\n"
        "v_exp_f32 v48, v48\n"
        "v_exp_f32 v49, v49\n"
        "v_exp_f32 v50, v50\n"
        "v_exp_f32 v51, v51\n"
        "v_cvt_pk_bf16_f32 v224, v48, v49\n"
        "v_cvt_pk_bf16_f32 v225, v50, v51\n"
        "v_mfma_f32_32x32x16_bf16 v[96:111], a[252:255], v[228:231], v[96:111]\n"
        "buffer_load_dwordx4 a[120:123], v30, s[40:43], 0 offen offset:3072 sc1\n"
        "v_exp_f32 v52, v52\n"
        "v_exp_f32 v53, v53\n"
        "v_exp_f32 v54, v54\n"
        "v_exp_f32 v55, v55\n"
        "v_cvt_pk_bf16_f32 v226, v52, v53\n"
        "v_cvt_pk_bf16_f32 v227, v54, v55\n"
        "v_mfma_f32_32x32x16_bf16 v[64:79], a[244:247], v[220:223], v[64:79]\n"
        "buffer_load_dwordx4 a[236:239], v29, s[12:15], 0 offen offset:1024 sc1\n"
        "v_exp_f32 v56, v56\n"
        "v_exp_f32 v57, v57\n"
        "v_exp_f32 v58, v58\n"
        "v_exp_f32 v59, v59\n"
        "v_cvt_pk_bf16_f32 v228, v56, v57\n"
        "v_cvt_pk_bf16_f32 v229, v58, v59\n"
        "v_mfma_f32_32x32x16_bf16 v[112:127], a[248:251], v[224:227], v[112:127]\n"
        "s_add_u32 s71, s71, 0xc00000\n"
        "v_exp_f32 v60, v60\n"
        "v_exp_f32 v61, v61\n"
        "v_exp_f32 v62, v62\n"
        "v_exp_f32 v63, v63\n"
        "v_cvt_pk_bf16_f32 v230, v60, v61\n"
        "v_cvt_pk_bf16_f32 v231, v62, v63\n"
        "v_mfma_f32_32x32x16_bf16 v[48:63], a[240:243], v[192:195], v[128:143]\n"
        "buffer_load_dwordx4 a[124:127], v31, s[40:43], 0 offen offset:3072 sc1\n"
        "v_exp_f32 v64, v64\n"
        "v_exp_f32 v65, v65\n"
        "v_exp_f32 v66, v66\n"
        "v_exp_f32 v67, v67\n"
        "v_cvt_pk_bf16_f32 v224, v64, v65\n"
        "v_cvt_pk_bf16_f32 v225, v66, v67\n"
        "v_mfma_f32_32x32x16_bf16 v[112:127], a[252:255], v[228:231], v[112:127]\n"
        "s_bfe_u32 s61, s20, 0x40010\n"
        "v_exp_f32 v68, v68\n"
        "v_exp_f32 v69, v69\n"
        "v_exp_f32 v70, v70\n"
        "v_exp_f32 v71, v71\n"
        "v_cvt_pk_bf16_f32 v226, v68, v69\n"
        "v_cvt_pk_bf16_f32 v227, v70, v71\n"
        "v_mfma_f32_32x32x16_bf16 v[48:63], a[244:247], v[196:199], v[48:63]\n"
        "s_lshl_b32 s61, s61, 12\n"
        "v_exp_f32 v72, v72\n"
        "v_exp_f32 v73, v73\n"
        "v_exp_f32 v74, v74\n"
        "v_exp_f32 v75, v75\n"
        "v_cvt_pk_bf16_f32 v228, v72, v73\n"
        "v_cvt_pk_bf16_f32 v229, v74, v75\n"
        "v_mfma_f32_32x32x16_bf16 v[232:247], a[248:251], v[224:227], v[232:247]\n"
        "s_waitcnt vmcnt(12)\n"
        "v_exp_f32 v76, v76\n"
        "v_exp_f32 v77, v77\n"
        "v_exp_f32 v78, v78\n"
        "v_exp_f32 v79, v79\n"
        "v_cvt_pk_bf16_f32 v230, v76, v77\n"
        "v_cvt_pk_bf16_f32 v231, v78, v79\n"
        "v_mfma_f32_32x32x16_bf16 v[64:79], v[32:35], v[200:203], v[144:159]\n"
        "s_add_u32 s62, s62, 0x10000\n"
        "v_exp_f32 v48, v48\n"
        "v_exp_f32 v49, v49\n"
        "v_exp_f32 v50, v50\n"
        "v_exp_f32 v51, v51\n"
        "v_cvt_pk_bf16_f32 v224, v48, v49\n"
        "v_cvt_pk_bf16_f32 v225, v50, v51\n"
        "v_mfma_f32_32x32x16_bf16 v[232:247], a[252:255], v[228:231], v[232:247]\n"
        "s_and_b32 s62, s62, 0xfffff\n"
        "v_exp_f32 v52, v52\n"
        "v_exp_f32 v53, v53\n"
        "v_exp_f32 v54, v54\n"
        "v_exp_f32 v55, v55\n"
        "v_cvt_pk_bf16_f32 v226, v52, v53\n"
        "v_cvt_pk_bf16_f32 v227, v54, v55\n"
        "v_mfma_f32_32x32x16_bf16 v[64:79], v[36:39], v[204:207], v[64:79]\n"
        "s_or_b32 s12, s95, s62\n"
        "v_exp_f32 v56, v56\n"
        "v_exp_f32 v57, v57\n"
        "v_exp_f32 v58, v58\n"
        "v_exp_f32 v59, v59\n"
        "v_cvt_pk_bf16_f32 v228, v56, v57\n"
        "v_cvt_pk_bf16_f32 v229, v58, v59\n"
        "v_mfma_f32_32x32x16_bf16 v[80:95], a[248:251], v[224:227], v[80:95]\n"
        "s_add_u32 s40, s18, s61\n"
        "s_addc_u32 s41, s19, 0\n"
        "v_exp_f32 v60, v60\n"
        "v_exp_f32 v61, v61\n"
        "v_exp_f32 v62, v62\n"
        "v_exp_f32 v63, v63\n"
        "v_cvt_pk_bf16_f32 v230, v60, v61\n"
        "v_cvt_pk_bf16_f32 v231, v62, v63\n"
        "v_mfma_f32_32x32x16_bf16 v[48:63], v[32:35], v[208:211], v[160:175]\n"
        "buffer_load_dwordx4 a[240:243], v28, s[12:15], 0 offen sc1\n"
        "v_exp_f32 v64, v64\n"
        "v_exp_f32 v65, v65\n"
        "v_exp_f32 v66, v66\n"
        "v_exp_f32 v67, v67\n"
        "v_cvt_pk_bf16_f32 v224, v64, v65\n"
        "v_cvt_pk_bf16_f32 v225, v66, v67\n"
        "v_mfma_f32_32x32x16_bf16 v[80:95], a[252:255], v[228:231], v[80:95]\n"
        "buffer_load_dwordx4 a[128:131], v30, s[40:43], 0 offen sc1\n"
        "v_exp_f32 v68, v68\n"
        "v_exp_f32 v69, v69\n"
        "v_exp_f32 v70, v70\n"
        "v_exp_f32 v71, v71\n"
        "v_cvt_pk_bf16_f32 v226, v68, v69\n"
        "v_cvt_pk_bf16_f32 v227, v70, v71\n"
        "v_mfma_f32_32x32x16_bf16 v[48:63], v[36:39], v[212:215], v[48:63]\n"
        "buffer_load_dwordx4 a[244:247], v28, s[12:15], 0 offen offset:1024 sc1\n"
        "v_exp_f32 v72, v72\n"
        "v_exp_f32 v73, v73\n"
        "v_exp_f32 v74, v74\n"
        "v_exp_f32 v75, v75\n"
        "v_cvt_pk_bf16_f32 v228, v72, v73\n"
        "v_cvt_pk_bf16_f32 v229, v74, v75\n"
        "v_mfma_f32_32x32x16_bf16 v[96:111], v[40:43], v[224:227], v[96:111]\n"
        "buffer_load_dwordx4 a[132:135], v31, s[40:43], 0 offen sc1\n"
        "v_exp_f32 v76, v76\n"
        "v_exp_f32 v77, v77\n"
        "v_exp_f32 v78, v78\n"
        "v_exp_f32 v79, v79\n"
        "v_cvt_pk_bf16_f32 v230, v76, v77\n"
        "v_cvt_pk_bf16_f32 v231, v78, v79\n"
        "v_mfma_f32_32x32x16_bf16 v[64:79], v[32:35], v[216:219], v[176:191]\n"
        "buffer_load_dwordx4 a[248:251], v29, s[12:15], 0 offen sc1\n"
        "v_exp_f32 v48, v48\n"
        "v_exp_f32 v49, v49\n"
        "v_exp_f32 v50, v50\n"
        "v_exp_f32 v51, v51\n"
        "v_cvt_pk_bf16_f32 v224, v48, v49\n"
        "v_cvt_pk_bf16_f32 v225, v50, v51\n"
        "v_mfma_f32_32x32x16_bf16 v[96:111], v[44:47], v[228:231], v[96:111]\n"
        "buffer_load_dwordx4 a[136:139], v30, s[40:43], 0 offen offset:1024 sc1\n"
        "v_exp_f32 v52, v52\n"
        "v_exp_f32 v53, v53\n"
        "v_exp_f32 v54, v54\n"
        "v_exp_f32 v55, v55\n"
        "v_cvt_pk_bf16_f32 v226, v52, v53\n"
        "v_cvt_pk_bf16_f32 v227, v54, v55\n"
        "v_mfma_f32_32x32x16_bf16 v[64:79], v[36:39], v[220:223], v[64:79]\n"
        "buffer_load_dwordx4 a[252:255], v29, s[12:15], 0 offen offset:1024 sc1\n"
        "v_exp_f32 v56, v56\n"
        "v_exp_f32 v57, v57\n"
        "v_exp_f32 v58, v58\n"
        "v_exp_f32 v59, v59\n"
        "v_cvt_pk_bf16_f32 v228, v56, v57\n"
        "v_cvt_pk_bf16_f32 v229, v58, v59\n"
        "v_mfma_f32_32x32x16_bf16 v[112:127], v[40:43], v[224:227], v[112:127]\n"
        "buffer_load_dwordx4 a[140:143], v31, s[40:43], 0 offen offset:1024 sc1\n"
        "v_exp_f32 v60, v60\n"
        "v_exp_f32 v61, v61\n"
        "v_exp_f32 v62, v62\n"
        "v_exp_f32 v63, v63\n"
        "v_cvt_pk_bf16_f32 v230, v60, v61\n"
        "v_cvt_pk_bf16_f32 v231, v62, v63\n"
        "v_mfma_f32_32x32x16_bf16 v[48:63], v[32:35], v[192:195], v[128:143]\n"
        "buffer_load_dwordx4 a[144:147], v30, s[40:43], 0 offen offset:2048 sc1\n"
        "v_exp_f32 v64, v64\n"
        "v_exp_f32 v65, v65\n"
        "v_exp_f32 v66, v66\n"
        "v_exp_f32 v67, v67\n"
        "v_cvt_pk_bf16_f32 v224, v64, v65\n"
        "v_cvt_pk_bf16_f32 v225, v66, v67\n"
        "v_mfma_f32_32x32x16_bf16 v[112:127], v[44:47], v[228:231], v[112:127]\n"
        "buffer_load_dwordx4 a[148:151], v31, s[40:43], 0 offen offset:2048 sc1\n"
        "v_exp_f32 v68, v68\n"
        "v_exp_f32 v69, v69\n"
        "v_exp_f32 v70, v70\n"
        "v_exp_f32 v71, v71\n"
        "v_cvt_pk_bf16_f32 v226, v68, v69\n"
        "v_cvt_pk_bf16_f32 v227, v70, v71\n"
        "v_mfma_f32_32x32x16_bf16 v[48:63], v[36:39], v[196:199], v[48:63]\n"
        "s_lshl_b32 s98, s78, 13\n"
        "v_exp_f32 v72, v72\n"
        "v_exp_f32 v73, v73\n"
        "v_exp_f32 v74, v74\n"
        "v_exp_f32 v75, v75\n"
        "v_cvt_pk_bf16_f32 v228, v72, v73\n"
        "v_cvt_pk_bf16_f32 v229, v74, v75\n"
        "v_mfma_f32_32x32x16_bf16 v[232:247], v[40:43], v[224:227], v[232:247]\n"
        "s_waitcnt vmcnt(11)\n"
        "v_exp_f32 v76, v76\n"
        "v_exp_f32 v77, v77\n"
        "v_exp_f32 v78, v78\n"
        "v_exp_f32 v79, v79\n"
        "v_cvt_pk_bf16_f32 v230, v76, v77\n"
        "v_cvt_pk_bf16_f32 v231, v78, v79\n"
        "v_mfma_f32_32x32x16_bf16 v[64:79], a[224:227], v[200:203], v[144:159]\n"
        "s_add_u32 s62, s62, 0x10000\n"
        "v_exp_f32 v48, v48\n"
        "v_exp_f32 v49, v49\n"
        "v_exp_f32 v50, v50\n"
        "v_exp_f32 v51, v51\n"
        "v_cvt_pk_bf16_f32 v224, v48, v49\n"
        "v_cvt_pk_bf16_f32 v225, v50, v51\n"
        "v_mfma_f32_32x32x16_bf16 v[232:247], v[44:47], v[228:231], v[232:247]\n"
        "s_and_b32 s62, s62, 0xfffff\n"
        "v_exp_f32 v52, v52\n"
        "v_exp_f32 v53, v53\n"
        "v_exp_f32 v54, v54\n"
        "v_exp_f32 v55, v55\n"
        "v_cvt_pk_bf16_f32 v226, v52, v53\n"
        "v_cvt_pk_bf16_f32 v227, v54, v55\n"
        "v_mfma_f32_32x32x16_bf16 v[64:79], a[228:231], v[204:207], v[64:79]\n"
        "s_or_b32 s12, s95, s62\n"
        "v_exp_f32 v56, v56\n"
        "v_exp_f32 v57, v57\n"
        "v_exp_f32 v58, v58\n"
        "v_exp_f32 v59, v59\n"
        "v_cvt_pk_bf16_f32 v228, v56, v57\n"
        "v_cvt_pk_bf16_f32 v229, v58, v59\n"
        "v_mfma_f32_32x32x16_bf16 v[80:95], v[40:43], v[224:227], v[80:95]\n"
        "buffer_load_dwordx4 a[152:155], v30, s[40:43], 0 offen offset:3072 sc1\n"
        "v_exp_f32 v60, v60\n"
        "v_exp_f32 v61, v61\n"
        "v_exp_f32 v62, v62\n"
        "v_exp_f32 v63, v63\n"
        "v_cvt_pk_bf16_f32 v230, v60, v61\n"
        "v_cvt_pk_bf16_f32 v231, v62, v63\n"
        "v_mfma_f32_32x32x16_bf16 v[48:63], a[224:227], v[208:211], v[160:175]\n"
        "buffer_load_dwordx4 v[32:35], v28, s[12:15], 0 offen sc1\n"
        "v_exp_f32 v64, v64\n"
        "v_exp_f32 v65, v65\n"
        "v_exp_f32 v66, v66\n"
        "v_exp_f32 v67, v67\n"
        "v_cvt_pk_bf16_f32 v224, v64, v65\n"
        "v_cvt_pk_bf16_f32 v225, v66, v67\n"
        "v_mfma_f32_32x32x16_bf16 v[80:95], v[44:47], v[228:231], v[80:95]\n"
        "buffer_load_dwordx4 a[156:159], v31, s[40:43], 0 offen offset:3072 sc1\n"
        "v_exp_f32 v68, v68\n"
        "v_exp_f32 v69, v69\n"
        "v_exp_f32 v70, v70\n"
        "v_exp_f32 v71, v71\n"
        "v_cvt_pk_bf16_f32 v226, v68, v69\n"
        "v_cvt_pk_bf16_f32 v227, v70, v71\n"
        "v_mfma_f32_32x32x16_bf16 v[48:63], a[228:231], v[212:215], v[48:63]\n"
        "buffer_load_dwordx4 v[36:39], v28, s[12:15], 0 offen offset:1024 sc1\n"
        "v_exp_f32 v72, v72\n"
        "v_exp_f32 v73, v73\n"
        "v_exp_f32 v74, v74\n"
        "v_exp_f32 v75, v75\n"
        "v_cvt_pk_bf16_f32 v228, v72, v73\n"
        "v_cvt_pk_bf16_f32 v229, v74, v75\n"
        "v_mfma_f32_32x32x16_bf16 v[96:111], a[232:235], v[224:227], v[96:111]\n"
        "s_bfe_u32 s61, s20, 0x40014\n"
        "v_exp_f32 v76, v76\n"
        "v_exp_f32 v77, v77\n"
        "v_exp_f32 v78, v78\n"
        "v_exp_f32 v79, v79\n"
        "v_cvt_pk_bf16_f32 v230, v76, v77\n"
        "v_cvt_pk_bf16_f32 v231, v78, v79\n"
        "v_mfma_f32_32x32x16_bf16 v[64:79], a[224:227], v[216:219], v[176:191]\n"
        "buffer_load_dwordx4 v[40:43], v29, s[12:15], 0 offen sc1\n"
        "v_exp_f32 v48, v48\n"
        "v_exp_f32 v49, v49\n"
        "v_exp_f32 v50, v50\n"
        "v_exp_f32 v51, v51\n"
        "v_cvt_pk_bf16_f32 v224, v48, v49\n"
        "v_cvt_pk_bf16_f32 v225, v50, v51\n"
        "v_mfma_f32_32x32x16_bf16 v[96:111], a[236:239], v[228:231], v[96:111]\n"
        "s_lshl_b32 s61, s61, 12\n"
        "v_exp_f32 v52, v52\n"
        "v_exp_f32 v53, v53\n"
        "v_exp_f32 v54, v54\n"
        "v_exp_f32 v55, v55\n"
        "v_cvt_pk_bf16_f32 v226, v52, v53\n"
        "v_cvt_pk_bf16_f32 v227, v54, v55\n"
        "v_mfma_f32_32x32x16_bf16 v[64:79], a[228:231], v[220:223], v[64:79]\n"
        "buffer_load_dwordx4 v[44:47], v29, s[12:15], 0 offen offset:1024 sc1\n"
        "v_exp_f32 v56, v56\n"
        "v_exp_f32 v57, v57\n"
        "v_exp_f32 v58, v58\n"
        "v_exp_f32 v59, v59\n"
        "v_cvt_pk_bf16_f32 v228, v56, v57\n"
        "v_cvt_pk_bf16_f32 v229, v58, v59\n"
        "v_mfma_f32_32x32x16_bf16 v[112:127], a[232:235], v[224:227], v[112:127]\n"
        "s_add_u32 s40, s18, s61\n"
        "s_addc_u32 s41, s19, 0\n"
        "v_exp_f32 v60, v60\n"
        "v_exp_f32 v61, v61\n"
        "v_exp_f32 v62, v62\n"
        "v_exp_f32 v63, v63\n"
        "v_cvt_pk_bf16_f32 v230, v60, v61\n"
        "v_cvt_pk_bf16_f32 v231, v62, v63\n"
        "v_mfma_f32_32x32x16_bf16 v[48:63], a[224:227], v[192:195], v[128:143]\n"
        "buffer_load_dwordx4 a[160:163], v30, s[40:43], 0 offen sc1\n"
        "v_exp_f32 v64, v64\n"
        "v_exp_f32 v65, v65\n"
        "v_exp_f32 v66, v66\n"
        "v_exp_f32 v67, v67\n"
        "v_cvt_pk_bf16_f32 v224, v64, v65\n"
        "v_cvt_pk_bf16_f32 v225, v66, v67\n"
        "v_mfma_f32_32x32x16_bf16 v[112:127], a[236:239], v[228:231], v[112:127]\n"
        "buffer_load_dwordx4 a[164:167], v31, s[40:43], 0 offen sc1\n"
        "v_exp_f32 v68, v68\n"
        "v_exp_f32 v69, v69\n"
        "v_exp_f32 v70, v70\n"
        "v_exp_f32 v71, v71\n"
        "v_cvt_pk_bf16_f32 v226, v68, v69\n"
        "v_cvt_pk_bf16_f32 v227, v70, v71\n"
        "v_mfma_f32_32x32x16_bf16 v[48:63], a[228:231], v[196:199], v[48:63]\n"
        "buffer_load_dwordx4 a[168:171], v30, s[40:43], 0 offen offset:1024 sc1\n"
        "v_exp_f32 v72, v72\n"
        "v_exp_f32 v73, v73\n"
        "v_exp_f32 v74, v74\n"
        "v_exp_f32 v75, v75\n"
        "v_cvt_pk_bf16_f32 v228, v72, v73\n"
        "v_cvt_pk_bf16_f32 v229, v74, v75\n"
        "v_mfma_f32_32x32x16_bf16 v[232:247], a[232:235], v[224:227], v[232:247]\n"
        "s_waitcnt vmcnt(12)\n"
        "v_exp_f32 v76, v76\n"
        "v_exp_f32 v77, v77\n"
        "v_exp_f32 v78, v78\n"
        "v_exp_f32 v79, v79\n"
        "v_cvt_pk_bf16_f32 v230, v76, v77\n"
        "v_cvt_pk_bf16_f32 v231, v78, v79\n"
        "v_mfma_f32_32x32x16_bf16 v[64:79], a[240:243], v[200:203], v[144:159]\n"
        "s_add_u32 s62, s62, 0x10000\n"
        "v_exp_f32 v48, v48\n"
        "v_exp_f32 v49, v49\n"
        "v_exp_f32 v50, v50\n"
        "v_exp_f32 v51, v51\n"
        "v_cvt_pk_bf16_f32 v224, v48, v49\n"
        "v_cvt_pk_bf16_f32 v225, v50, v51\n"
        "v_mfma_f32_32x32x16_bf16 v[232:247], a[236:239], v[228:231], v[232:247]\n"
        "s_and_b32 s62, s62, 0xfffff\n"
        "v_exp_f32 v52, v52\n"
        "v_exp_f32 v53, v53\n"
        "v_exp_f32 v54, v54\n"
        "v_exp_f32 v55, v55\n"
        "v_cvt_pk_bf16_f32 v226, v52, v53\n"
        "v_cvt_pk_bf16_f32 v227, v54, v55\n"
        "v_mfma_f32_32x32x16_bf16 v[64:79], a[244:247], v[204:207], v[64:79]\n"
        "s_or_b32 s12, s95, s62\n"
        "v_exp_f32 v56, v56\n"
        "v_exp_f32 v57, v57\n"
        "v_exp_f32 v58, v58\n"
        "v_exp_f32 v59, v59\n"
        "v_cvt_pk_bf16_f32 v228, v56, v57\n"
        "v_cvt_pk_bf16_f32 v229, v58, v59\n"
        "v_mfma_f32_32x32x16_bf16 v[80:95], a[232:235], v[224:227], v[80:95]\n"
        "buffer_load_dwordx4 a[172:175], v31, s[40:43], 0 offen offset:1024 sc1\n"
        "v_exp_f32 v60, v60\n"
        "v_exp_f32 v61, v61\n"
        "v_exp_f32 v62, v62\n"
        "v_exp_f32 v63, v63\n"
        "v_cvt_pk_bf16_f32 v230, v60, v61\n"
        "v_cvt_pk_bf16_f32 v231, v62, v63\n"
        "v_mfma_f32_32x32x16_bf16 v[48:63], a[240:243], v[208:211], v[160:175]\n"
        "buffer_load_dwordx4 a[224:227], v28, s[12:15], 0 offen sc1\n"
        "v_exp_f32 v64, v64\n"
        "v_exp_f32 v65, v65\n"
        "v_exp_f32 v66, v66\n"
        "v_exp_f32 v67, v67\n"
        "v_cvt_pk_bf16_f32 v224, v64, v65\n"
        "v_cvt_pk_bf16_f32 v225, v66, v67\n"
        "v_mfma_f32_32x32x16_bf16 v[80:95], a[236:239], v[228:231], v[80:95]\n"
        "buffer_load_dwordx4 a[176:179], v30, s[40:43], 0 offen offset:2048 sc1\n"
        "v_exp_f32 v68, v68\n"
        "v_exp_f32 v69, v69\n"
        "v_exp_f32 v70, v70\n"
        "v_exp_f32 v71, v71\n"
        "v_cvt_pk_bf16_f32 v226, v68, v69\n"
        "v_cvt_pk_bf16_f32 v227, v70, v71\n"
        "v_mfma_f32_32x32x16_bf16 v[48:63], a[244:247], v[212:215], v[48:63]\n"
        "buffer_load_dwordx4 a[228:231], v28, s[12:15], 0 offen offset:1024 sc1\n"
        "v_exp_f32 v72, v72\n"
        "v_exp_f32 v73, v73\n"
        "v_exp_f32 v74, v74\n"
        "v_exp_f32 v75, v75\n"
        "v_cvt_pk_bf16_f32 v228, v72, v73\n"
        "v_cvt_pk_bf16_f32 v229, v74, v75\n"
        "v_mfma_f32_32x32x16_bf16 v[96:111], a[248:251], v[224:227], v[96:111]\n"
        "buffer_load_dwordx4 a[180:183], v31, s[40:43], 0 offen offset:2048 sc1\n"
        "v_exp_f32 v76, v76\n"
        "v_exp_f32 v77, v77\n"
        "v_exp_f32 v78, v78\n"
        "v_exp_f32 v79, v79\n"
        "v_cvt_pk_bf16_f32 v230, v76, v77\n"
        "v_cvt_pk_bf16_f32 v231, v78, v79\n"
        "v_mfma_f32_32x32x16_bf16 v[64:79], a[240:243], v[216:219], v[176:191]\n"
        "buffer_load_dwordx4 a[232:235], v29, s[12:15], 0 offen sc1\n"
        "v_exp_f32 v48, v48\n"
        "v_exp_f32 v49, v49\n"
        "v_exp_f32 v50, v50\n"
        "v_exp_f32 v51, v51\n"
        "v_cvt_pk_bf16_f32 v224, v48, v49\n"
        "v_cvt_pk_bf16_f32 v225, v50, v51\n"
        "v_mfma_f32_32x32x16_bf16 v[96:111], a[252:255], v[228:231], v[96:111]\n"
        "buffer_load_dwordx4 a[184:187], v30, s[40:43], 0 offen offset:3072 sc1\n"
        "v_exp_f32 v52, v52\n"
        "v_exp_f32 v53, v53\n"
        "v_exp_f32 v54, v54\n"
        "v_exp_f32 v55, v55\n"
        "v_cvt_pk_bf16_f32 v226, v52, v53\n"
        "v_cvt_pk_bf16_f32 v227, v54, v55\n"
        "v_mfma_f32_32x32x16_bf16 v[64:79], a[244:247], v[220:223], v[64:79]\n"
        "buffer_load_dwordx4 a[236:239], v29, s[12:15], 0 offen offset:1024 sc1\n"
        "v_exp_f32 v56, v56\n"
        "v_exp_f32 v57, v57\n"
        "v_exp_f32 v58, v58\n"
        "v_exp_f32 v59, v59\n"
        "v_cvt_pk_bf16_f32 v228, v56, v57\n"
        "v_cvt_pk_bf16_f32 v229, v58, v59\n"
        "v_mfma_f32_32x32x16_bf16 v[112:127], a[248:251], v[224:227], v[112:127]\n"
        "s_add_u32 s98, s98, 0xc00000\n"
        "v_exp_f32 v60, v60\n"
        "v_exp_f32 v61, v61\n"
        "v_exp_f32 v62, v62\n"
        "v_exp_f32 v63, v63\n"
        "v_cvt_pk_bf16_f32 v230, v60, v61\n"
        "v_cvt_pk_bf16_f32 v231, v62, v63\n"
        "v_mfma_f32_32x32x16_bf16 v[48:63], a[240:243], v[192:195], v[128:143]\n"
        "buffer_load_dwordx4 a[188:191], v31, s[40:43], 0 offen offset:3072 sc1\n"
        "v_exp_f32 v64, v64\n"
        "v_exp_f32 v65, v65\n"
        "v_exp_f32 v66, v66\n"
        "v_exp_f32 v67, v67\n"
        "v_cvt_pk_bf16_f32 v224, v64, v65\n"
        "v_cvt_pk_bf16_f32 v225, v66, v67\n"
        "v_mfma_f32_32x32x16_bf16 v[112:127], a[252:255], v[228:231], v[112:127]\n"
        "s_bfe_u32 s61, s20, 0x40018\n"
        "v_exp_f32 v68, v68\n"
        "v_exp_f32 v69, v69\n"
        "v_exp_f32 v70, v70\n"
        "v_exp_f32 v71, v71\n"
        "v_cvt_pk_bf16_f32 v226, v68, v69\n"
        "v_cvt_pk_bf16_f32 v227, v70, v71\n"
        "v_mfma_f32_32x32x16_bf16 v[48:63], a[244:247], v[196:199], v[48:63]\n"
        "s_lshl_b32 s61, s61, 12\n"
        "v_exp_f32 v72, v72\n"
        "v_exp_f32 v73, v73\n"
        "v_exp_f32 v74, v74\n"
        "v_exp_f32 v75, v75\n"
        "v_cvt_pk_bf16_f32 v228, v72, v73\n"
        "v_cvt_pk_bf16_f32 v229, v74, v75\n"
        "v_mfma_f32_32x32x16_bf16 v[232:247], a[248:251], v[224:227], v[232:247]\n"
        "s_waitcnt vmcnt(12)\n"
        "v_exp_f32 v76, v76\n"
        "v_exp_f32 v77, v77\n"
        "v_exp_f32 v78, v78\n"
        "v_exp_f32 v79, v79\n"
        "v_cvt_pk_bf16_f32 v230, v76, v77\n"
        "v_cvt_pk_bf16_f32 v231, v78, v79\n"
        "v_mfma_f32_32x32x16_bf16 v[64:79], v[32:35], v[200:203], v[144:159]\n"
        "s_add_u32 s40, s18, s61\n"
        "s_addc_u32 s41, s19, 0\n"
        "v_exp_f32 v48, v48\n"
        "v_exp_f32 v49, v49\n"
        "v_exp_f32 v50, v50\n"
        "v_exp_f32 v51, v51\n"
        "v_cvt_pk_bf16_f32 v224, v48, v49\n"
        "v_cvt_pk_bf16_f32 v225, v50, v51\n"
        "v_mfma_f32_32x32x16_bf16 v[232:247], a[252:255], v[228:231], v[232:247]\n"
        "buffer_load_dwordx4 a[192:195], v30, s[40:43], 0 offen sc1\n"
        "v_exp_f32 v52, v52\n"
        "v_exp_f32 v53, v53\n"
        "v_exp_f32 v54, v54\n"
        "v_exp_f32 v55, v55\n"
        "v_cvt_pk_bf16_f32 v226, v52, v53\n"
        "v_cvt_pk_bf16_f32 v227, v54, v55\n"
        "v_mfma_f32_32x32x16_bf16 v[64:79], v[36:39], v[204:207], v[64:79]\n"
        "buffer_load_dwordx4 a[196:199], v31, s[40:43], 0 offen sc1\n"
        "v_exp_f32 v56, v56\n"
        "v_exp_f32 v57, v57\n"
        "v_exp_f32 v58, v58\n"
        "v_exp_f32 v59, v59\n"
        "v_cvt_pk_bf16_f32 v228, v56, v57\n"
        "v_cvt_pk_bf16_f32 v229, v58, v59\n"
        "v_mfma_f32_32x32x16_bf16 v[80:95], a[248:251], v[224:227], v[80:95]\n"
        "s_lshl_b32 s72, s99, 15\n"
        "v_exp_f32 v60, v60\n"
        "v_exp_f32 v61, v61\n"
        "v_exp_f32 v62, v62\n"
        "v_exp_f32 v63, v63\n"
        "v_cvt_pk_bf16_f32 v230, v60, v61\n"
        "v_cvt_pk_bf16_f32 v231, v62, v63\n"
        "v_mfma_f32_32x32x16_bf16 v[48:63], v[32:35], v[208:211], v[160:175]\n"
        "buffer_load_dwordx4 a[200:203], v30, s[40:43], 0 offen offset:1024 sc1\n"
        "v_exp_f32 v64, v64\n"
        "v_exp_f32 v65, v65\n"
        "v_exp_f32 v66, v66\n"
        "v_exp_f32 v67, v67\n"
        "v_cvt_pk_bf16_f32 v224, v64, v65\n"
        "v_cvt_pk_bf16_f32 v225, v66, v67\n"
        "v_mfma_f32_32x32x16_bf16 v[80:95], a[252:255], v[228:231], v[80:95]\n"
        "s_add_u32 s72, s72, s98\n"
        "v_exp_f32 v68, v68\n"
        "v_exp_f32 v69, v69\n"
        "v_exp_f32 v70, v70\n"
        "v_exp_f32 v71, v71\n"
        "v_cvt_pk_bf16_f32 v226, v68, v69\n"
        "v_cvt_pk_bf16_f32 v227, v70, v71\n"
        "v_mfma_f32_32x32x16_bf16 v[48:63], v[36:39], v[212:215], v[48:63]\n"
        "s_xor_b32 s73, s72, 0x8000\n"
        "v_exp_f32 v72, v72\n"
        "v_exp_f32 v73, v73\n"
        "v_exp_f32 v74, v74\n"
        "v_exp_f32 v75, v75\n"
        "v_cvt_pk_bf16_f32 v228, v72, v73\n"
        "v_cvt_pk_bf16_f32 v229, v74, v75\n"
        "v_mfma_f32_32x32x16_bf16 v[96:111], v[40:43], v[224:227], v[96:111]\n"
        "s_and_b32 s98, s78, 1\n"
        "v_exp_f32 v76, v76\n"
        "v_exp_f32 v77, v77\n"
        "v_exp_f32 v78, v78\n"
        "v_exp_f32 v79, v79\n"
        "v_cvt_pk_bf16_f32 v230, v76, v77\n"
        "v_cvt_pk_bf16_f32 v231, v78, v79\n"
        "v_mfma_f32_32x32x16_bf16 v[64:79], v[32:35], v[216:219], v[176:191]\n"
        "buffer_load_dwordx4 a[204:207], v31, s[40:43], 0 offen offset:1024 sc1\n"
        "v_exp_f32 v48, v48\n"
        "v_exp_f32 v49, v49\n"
        "v_exp_f32 v50, v50\n"
        "v_exp_f32 v51, v51\n"
        "v_cvt_pk_bf16_f32 v224, v48, v49\n"
        "v_cvt_pk_bf16_f32 v225, v50, v51\n"
        "v_mfma_f32_32x32x16_bf16 v[96:111], v[44:47], v[228:231], v[96:111]\n"
        "s_lshl_b32 s98, s98, 6\n"
        "v_exp_f32 v52, v52\n"
        "v_exp_f32 v53, v53\n"
        "v_exp_f32 v54, v54\n"
        "v_exp_f32 v55, v55\n"
        "v_cvt_pk_bf16_f32 v226, v52, v53\n"
        "v_cvt_pk_bf16_f32 v227, v54, v55\n"
        "v_mfma_f32_32x32x16_bf16 v[64:79], v[36:39], v[220:223], v[64:79]\n"
        "s_add_u32 s87, s67, s98\n"
        "v_exp_f32 v56, v56\n"
        "v_exp_f32 v57, v57\n"
        "v_exp_f32 v58, v58\n"
        "v_exp_f32 v59, v59\n"
        "v_cvt_pk_bf16_f32 v228, v56, v57\n"
        "v_cvt_pk_bf16_f32 v229, v58, v59\n"
        "v_mfma_f32_32x32x16_bf16 v[112:127], v[40:43], v[224:227], v[112:127]\n"
        "s_mul_i32 s74, s99, 28672\n"
        "v_exp_f32 v60, v60\n"
        "v_exp_f32 v61, v61\n"
        "v_exp_f32 v62, v62\n"
        "v_exp_f32 v63, v63\n"
        "v_cvt_pk_bf16_f32 v230, v60, v61\n"
        "v_cvt_pk_bf16_f32 v231, v62, v63\n"
        "v_mfma_f32_32x32x16_bf16 v[48:63], v[32:35], v[192:195], v[128:143]\n"
        "buffer_load_dwordx4 a[208:211], v30, s[40:43], 0 offen offset:2048 sc1\n"
        "v_exp_f32 v64, v64\n"
        "v_exp_f32 v65, v65\n"
        "v_exp_f32 v66, v66\n"
        "v_exp_f32 v67, v67\n"
        "v_cvt_pk_bf16_f32 v224, v64, v65\n"
        "v_cvt_pk_bf16_f32 v225, v66, v67\n"
        "v_mfma_f32_32x32x16_bf16 v[112:127], v[44:47], v[228:231], v[112:127]\n"
        "buffer_load_dwordx4 a[212:215], v31, s[40:43], 0 offen offset:2048 sc1\n"
        "v_exp_f32 v68, v68\n"
        "v_exp_f32 v69, v69\n"
        "v_exp_f32 v70, v70\n"
        "v_exp_f32 v71, v71\n"
        "v_cvt_pk_bf16_f32 v226, v68, v69\n"
        "v_cvt_pk_bf16_f32 v227, v70, v71\n"
        "v_mfma_f32_32x32x16_bf16 v[48:63], v[36:39], v[196:199], v[48:63]\n"
        "s_add_u32 s74, s74, s87\n"
        "v_exp_f32 v72, v72\n"
        "v_exp_f32 v73, v73\n"
        "v_exp_f32 v74, v74\n"
        "v_exp_f32 v75, v75\n"
        "v_cvt_pk_bf16_f32 v228, v72, v73\n"
        "v_cvt_pk_bf16_f32 v229, v74, v75\n"
        "v_mfma_f32_32x32x16_bf16 v[232:247], v[40:43], v[224:227], v[232:247]\n"
        "s_waitcnt vmcnt(7)\n"
        "v_exp_f32 v76, v76\n"
        "v_exp_f32 v77, v77\n"
        "v_exp_f32 v78, v78\n"
        "v_exp_f32 v79, v79\n"
        "v_cvt_pk_bf16_f32 v230, v76, v77\n"
        "v_cvt_pk_bf16_f32 v231, v78, v79\n"
        "v_mfma_f32_32x32x16_bf16 v[64:79], a[224:227], v[200:203], v[144:159]\n"
        "buffer_load_dwordx4 a[216:219], v30, s[40:43], 0 offen offset:3072 sc1\n"
        "v_exp_f32 v48, v48\n"
        "v_exp_f32 v49, v49\n"
        "v_exp_f32 v50, v50\n"
        "v_exp_f32 v51, v51\n"
        "v_cvt_pk_bf16_f32 v224, v48, v49\n"
        "v_cvt_pk_bf16_f32 v225, v50, v51\n"
        "v_mfma_f32_32x32x16_bf16 v[232:247], v[44:47], v[228:231], v[232:247]\n"
        "s_mul_i32 s98, s78, 100352\n"
        "v_exp_f32 v52, v52\n"
        "v_exp_f32 v53, v53\n"
        "v_exp_f32 v54, v54\n"
        "v_exp_f32 v55, v55\n"
        "v_cvt_pk_bf16_f32 v226, v52, v53\n"
        "v_cvt_pk_bf16_f32 v227, v54, v55\n"
        "v_mfma_f32_32x32x16_bf16 v[64:79], a[228:231], v[204:207], v[64:79]\n"
        "s_add_u32 s67, s67, s98\n"
        "v_exp_f32 v56, v56\n"
        "v_exp_f32 v57, v57\n"
        "v_exp_f32 v58, v58\n"
        "v_exp_f32 v59, v59\n"
        "v_cvt_pk_bf16_f32 v228, v56, v57\n"
        "v_cvt_pk_bf16_f32 v229, v58, v59\n"
        "v_mfma_f32_32x32x16_bf16 v[80:95], v[40:43], v[224:227], v[80:95]\n"
        "buffer_load_dwordx2 v[2:3], v248, s[8:11], s74 offen offset:0\n"
        "v_exp_f32 v60, v60\n"
        "v_exp_f32 v61, v61\n"
        "v_exp_f32 v62, v62\n"
        "v_exp_f32 v63, v63\n"
        "v_cvt_pk_bf16_f32 v230, v60, v61\n"
        "v_cvt_pk_bf16_f32 v231, v62, v63\n"
        "v_mfma_f32_32x32x16_bf16 v[48:63], a[224:227], v[208:211], v[160:175]\n"
        "buffer_load_dwordx4 a[220:223], v31, s[40:43], 0 offen offset:3072 sc1\n"
        "v_exp_f32 v64, v64\n"
        "v_exp_f32 v65, v65\n"
        "v_exp_f32 v66, v66\n"
        "v_exp_f32 v67, v67\n"
        "v_cvt_pk_bf16_f32 v224, v64, v65\n"
        "v_cvt_pk_bf16_f32 v225, v66, v67\n"
        "v_mfma_f32_32x32x16_bf16 v[80:95], v[44:47], v[228:231], v[80:95]\n"
        "buffer_load_dwordx2 v[18:19], v249, s[52:55], s87 offen offset:0\n"
        "v_exp_f32 v68, v68\n"
        "v_exp_f32 v69, v69\n"
        "v_exp_f32 v70, v70\n"
        "v_exp_f32 v71, v71\n"
        "v_cvt_pk_bf16_f32 v226, v68, v69\n"
        "v_cvt_pk_bf16_f32 v227, v70, v71\n"
        "v_mfma_f32_32x32x16_bf16 v[48:63], a[228:231], v[212:215], v[48:63]\n"
        "buffer_load_dwordx2 v[4:5], v248, s[8:11], s74 offen offset:16\n"
        "v_exp_f32 v72, v72\n"
        "v_exp_f32 v73, v73\n"
        "v_exp_f32 v74, v74\n"
        "v_exp_f32 v75, v75\n"
        "v_cvt_pk_bf16_f32 v228, v72, v73\n"
        "v_cvt_pk_bf16_f32 v229, v74, v75\n"
        "v_mfma_f32_32x32x16_bf16 v[96:111], a[232:235], v[224:227], v[96:111]\n"
        "buffer_load_dwordx2 v[20:21], v249, s[52:55], s87 offen offset:16\n"
        "v_exp_f32 v76, v76\n"
        "v_exp_f32 v77, v77\n"
        "v_exp_f32 v78, v78\n"
        "v_exp_f32 v79, v79\n"
        "v_cvt_pk_bf16_f32 v230, v76, v77\n"
        "v_cvt_pk_bf16_f32 v231, v78, v79\n"
        "v_mfma_f32_32x32x16_bf16 v[64:79], a[224:227], v[216:219], v[176:191]\n"
        "buffer_load_dwordx2 v[10:11], v248, s[8:11], s74 offen offset:32\n"
        "v_exp_f32 v48, v48\n"
        "v_exp_f32 v49, v49\n"
        "v_exp_f32 v50, v50\n"
        "v_exp_f32 v51, v51\n"
        "v_cvt_pk_bf16_f32 v224, v48, v49\n"
        "v_cvt_pk_bf16_f32 v225, v50, v51\n"
        "v_mfma_f32_32x32x16_bf16 v[96:111], a[236:239], v[228:231], v[96:111]\n"
        "buffer_load_dwordx2 v[252:253], v249, s[52:55], s87 offen offset:32\n"
        "v_exp_f32 v52, v52\n"
        "v_exp_f32 v53, v53\n"
        "v_exp_f32 v54, v54\n"
        "v_exp_f32 v55, v55\n"
        "v_cvt_pk_bf16_f32 v226, v52, v53\n"
        "v_cvt_pk_bf16_f32 v227, v54, v55\n"
        "v_mfma_f32_32x32x16_bf16 v[64:79], a[228:231], v[220:223], v[64:79]\n"
        "buffer_load_dwordx2 v[12:13], v248, s[8:11], s74 offen offset:48\n"
        "v_exp_f32 v56, v56\n"
        "v_exp_f32 v57, v57\n"
        "v_exp_f32 v58, v58\n"
        "v_exp_f32 v59, v59\n"
        "v_cvt_pk_bf16_f32 v228, v56, v57\n"
        "v_cvt_pk_bf16_f32 v229, v58, v59\n"
        "v_mfma_f32_32x32x16_bf16 v[112:127], a[232:235], v[224:227], v[112:127]\n"
        "buffer_load_dwordx2 v[254:255], v249, s[52:55], s87 offen offset:48\n"
        "v_exp_f32 v60, v60\n"
        "v_exp_f32 v61, v61\n"
        "v_exp_f32 v62, v62\n"
        "v_exp_f32 v63, v63\n"
        "v_cvt_pk_bf16_f32 v230, v60, v61\n"
        "v_cvt_pk_bf16_f32 v231, v62, v63\n"
        "v_mfma_f32_32x32x16_bf16 v[48:63], a[224:227], v[192:195], v[128:143]\n"
        "ds_write_b128 v6, v[96:99] offset:28672\n"
        "v_exp_f32 v64, v64\n"
        "v_exp_f32 v65, v65\n"
        "v_exp_f32 v66, v66\n"
        "v_exp_f32 v67, v67\n"
        "v_cvt_pk_bf16_f32 v224, v64, v65\n"
        "v_cvt_pk_bf16_f32 v225, v66, v67\n"
        "v_mfma_f32_32x32x16_bf16 v[112:127], a[236:239], v[228:231], v[112:127]\n"
        "ds_write_b128 v6, v[100:103] offset:29696\n"
        "v_exp_f32 v68, v68\n"
        "v_exp_f32 v69, v69\n"
        "v_exp_f32 v70, v70\n"
        "v_exp_f32 v71, v71\n"
        "v_cvt_pk_bf16_f32 v226, v68, v69\n"
        "v_cvt_pk_bf16_f32 v227, v70, v71\n"
        "v_mfma_f32_32x32x16_bf16 v[48:63], a[228:231], v[196:199], v[48:63]\n"
        "ds_write_b128 v6, v[104:107] offset:30720\n"
        "v_exp_f32 v72, v72\n"
        "v_exp_f32 v73, v73\n"
        "v_exp_f32 v74, v74\n"
        "v_exp_f32 v75, v75\n"
        "v_cvt_pk_bf16_f32 v228, v72, v73\n"
        "v_cvt_pk_bf16_f32 v229, v74, v75\n"
        "v_mfma_f32_32x32x16_bf16 v[232:247], a[232:235], v[224:227], v[232:247]\n"
        "ds_write_b128 v6, v[108:111] offset:31744\n"
        "v_exp_f32 v76, v76\n"
        "v_exp_f32 v77, v77\n"
        "v_exp_f32 v78, v78\n"
        "v_exp_f32 v79, v79\n"
        "v_cvt_pk_bf16_f32 v230, v76, v77\n"
        "v_cvt_pk_bf16_f32 v231, v78, v79\n"
        "ds_write_b128 v6, v[112:115] offset:32768\n"
        "v_exp_f32 v48, v48\n"
        "v_exp_f32 v49, v49\n"
        "v_exp_f32 v50, v50\n"
        "v_exp_f32 v51, v51\n"
        "v_cvt_pk_bf16_f32 v224, v48, v49\n"
        "v_cvt_pk_bf16_f32 v225, v50, v51\n"
        "v_mfma_f32_32x32x16_bf16 v[232:247], a[236:239], v[228:231], v[232:247]\n"
        "ds_write_b128 v6, v[116:119] offset:33792\n"
        "v_exp_f32 v52, v52\n"
        "v_exp_f32 v53, v53\n"
        "v_exp_f32 v54, v54\n"
        "v_exp_f32 v55, v55\n"
        "v_cvt_pk_bf16_f32 v226, v52, v53\n"
        "v_cvt_pk_bf16_f32 v227, v54, v55\n"
        "ds_write_b128 v6, v[120:123] offset:34816\n"
        "v_exp_f32 v56, v56\n"
        "v_exp_f32 v57, v57\n"
        "v_exp_f32 v58, v58\n"
        "v_exp_f32 v59, v59\n"
        "v_cvt_pk_bf16_f32 v228, v56, v57\n"
        "v_cvt_pk_bf16_f32 v229, v58, v59\n"
        "v_mfma_f32_32x32x16_bf16 v[80:95], a[232:235], v[224:227], v[80:95]\n"
        "ds_write_b128 v6, v[124:127] offset:35840\n"
        "v_exp_f32 v60, v60\n"
        "v_exp_f32 v61, v61\n"
        "v_exp_f32 v62, v62\n"
        "v_exp_f32 v63, v63\n"
        "v_cvt_pk_bf16_f32 v230, v60, v61\n"
        "v_cvt_pk_bf16_f32 v231, v62, v63\n"
        "ds_write_b128 v6, v[232:235] offset:36864\n"
        "ds_write_b128 v6, v[236:239] offset:37888\n"
        "v_mfma_f32_32x32x16_bf16 v[80:95], a[236:239], v[228:231], v[80:95]\n"
        "ds_write_b128 v6, v[240:243] offset:38912\n"
        "ds_write_b128 v6, v[244:247] offset:39936\n"
        "s_waitcnt lgkmcnt(0)\n"
        "s_barrier\n"
        "ds_read_b128 v[144:147], v9 offset:28672\n"
        "ds_read_b128 v[148:151], v9 offset:29696\n"
        "ds_read_b128 v[152:155], v9 offset:30720\n"
        "ds_read_b128 v[156:159], v9 offset:31744\n"
        "ds_read_b128 v[160:163], v8 offset:32768\n"
        "ds_read_b128 v[164:167], v8 offset:33792\n"
        "ds_read_b128 v[168:171], v8 offset:34816\n"
        "ds_read_b128 v[172:175], v8 offset:35840\n"
        "ds_read_b128 v[176:179], v7 offset:36864\n"
        "ds_read_b128 v[180:183], v7 offset:37888\n"
        "ds_read_b128 v[184:187], v7 offset:38912\n"
        "ds_read_b128 v[188:191], v7 offset:39936\n"
        "s_waitcnt lgkmcnt(10)\n"
        "v_pk_add_f32 v[80:81], v[80:81], v[144:145]\n"
        "v_pk_add_f32 v[82:83], v[82:83], v[146:147]\n"
        "v_pk_add_f32 v[84:85], v[84:85], v[148:149]\n"
        "v_pk_add_f32 v[86:87], v[86:87], v[150:151]\n"
        "s_waitcnt lgkmcnt(8)\n"
        "v_pk_add_f32 v[88:89], v[88:89], v[152:153]\n"
        "v_pk_add_f32 v[90:91], v[90:91], v[154:155]\n"
        "v_pk_add_f32 v[92:93], v[92:93], v[156:157]\n"
        "v_pk_add_f32 v[94:95], v[94:95], v[158:159]\n"
        "s_waitcnt lgkmcnt(6)\n"
        "v_pk_add_f32 v[80:81], v[80:81], v[160:161]\n"
        "v_pk_add_f32 v[82:83], v[82:83], v[162:163]\n"
        "v_pk_add_f32 v[84:85], v[84:85], v[164:165]\n"
        "v_pk_add_f32 v[86:87], v[86:87], v[166:167]\n"
        "s_waitcnt lgkmcnt(4)\n"
        "v_pk_add_f32 v[88:89], v[88:89], v[168:169]\n"
        "v_pk_add_f32 v[90:91], v[90:91], v[170:171]\n"
        "v_pk_add_f32 v[92:93], v[92:93], v[172:173]\n"
        "v_pk_add_f32 v[94:95], v[94:95], v[174:175]\n"
        "s_waitcnt lgkmcnt(2)\n"
        "v_pk_add_f32 v[80:81], v[80:81], v[176:177]\n"
        "v_pk_add_f32 v[82:83], v[82:83], v[178:179]\n"
        "v_pk_add_f32 v[84:85], v[84:85], v[180:181]\n"
        "v_pk_add_f32 v[86:87], v[86:87], v[182:183]\n"
        "s_waitcnt lgkmcnt(0)\n"
        "v_pk_add_f32 v[88:89], v[88:89], v[184:185]\n"
        "v_pk_add_f32 v[90:91], v[90:91], v[186:187]\n"
        "v_pk_add_f32 v[92:93], v[92:93], v[188:189]\n"
        "v_pk_add_f32 v[94:95], v[94:95], v[190:191]\n"
        "v_pk_add_f32 v[48:49], v[80:81], v[88:89]\n"
        "v_pk_add_f32 v[50:51], v[82:83], v[90:91]\n"
        "v_pk_add_f32 v[52:53], v[84:85], v[92:93]\n"
        "v_pk_add_f32 v[54:55], v[86:87], v[94:95]\n"
        "v_mov_b32 v26, v92\n"
        "v_mov_b32 v30, v92\n"
        "v_pk_add_f32 v[48:49], v[48:49], v[52:53]\n"
        "v_pk_add_f32 v[50:51], v[50:51], v[54:55]\n"
        "v_permlane32_swap_b32 v26, v30\n"
        "v_pk_add_f32 v[48:49], v[48:49], v[50:51]\n"
        "v_cmp_ngt_f32 vcc, 0x7b800000, v30\n"
        "v_add_f32 v48, v48, v49\n"
        "s_movk_i32 s98, 0x207\n"
        "v_rcp_f32 v30, v30\n"
        "v_cmp_class_f32 s[96:97], v48, s98\n"
        "s_nop 0\n"
        "s_or_b64 vcc, vcc, s[96:97]\n"
        "s_cbranch_vccnz fb_%=\n"
        "norm_%=:\n"
        "v_pk_mul_f32 v[80:81], v[80:81], v[30:31] op_sel_hi:[1,0]\n"
        "v_pk_mul_f32 v[82:83], v[82:83], v[30:31] op_sel_hi:[1,0]\n"
        "v_pk_mul_f32 v[84:85], v[84:85], v[30:31] op_sel_hi:[1,0]\n"
        "v_pk_mul_f32 v[86:87], v[86:87], v[30:31] op_sel_hi:[1,0]\n"
        "v_pk_mul_f32 v[88:89], v[88:89], v[30:31] op_sel_hi:[1,0]\n"
        "v_pk_mul_f32 v[90:91], v[90:91], v[30:31] op_sel_hi:[1,0]\n"
        "v_pk_mul_f32 v[92:93], v[92:93], v[30:31] op_sel_hi:[1,0]\n"
        "v_pk_mul_f32 v[94:95], v[94:95], v[30:31] op_sel_hi:[1,0]\n"
        "v_cvt_pk_bf16_f32 v224, v80, v81\n"
        "v_cvt_pk_bf16_f32 v225, v82, v83\n"
        "v_cvt_pk_bf16_f32 v226, v84, v85\n"
        "v_cvt_pk_bf16_f32 v227, v86, v87\n"
        "buffer_store_dwordx4 v[224:227], v1, s[28:31], s71 offen sc1\n"
        "v_cvt_pk_bf16_f32 v228, v88, v89\n"
        "v_cvt_pk_bf16_f32 v229, v90, v91\n"
        "v_cvt_pk_bf16_f32 v230, v92, v93\n"
        "v_cvt_pk_bf16_f32 v231, v94, v95\n"
        "v_cndmask_b32 v230, v230, 0, s[92:93]\n"
        "v_cndmask_b32 v231, v231, 0, s[92:93]\n"
        "buffer_store_dwordx4 v[228:231], v1, s[28:31], s71 offen offset:1024 sc1\n"
        "s_barrier\n"
        "s_cmp_eq_u32 s86, 7\n"
        "s_cbranch_scc1 tail_%=\n"
        "v_bfe_u32 v28, v1, 6, 2\n"
        "v_lshrrev_b32 v29, 9, v1\n"
        "v_lshl_add_u32 v27, v29, 2, v28\n"
        "v_lshlrev_b32 v27, 7, v27\n"
        "v_lshrrev_b32 v28, 1, v28\n"
        "s_and_b32 s96, s78, 1\n"
        "v_xor_b32 v28, s96, v28\n"
        "v_lshl_add_u32 v27, v28, 6, v27\n"
        "v_bfe_u32 v28, v1, 8, 1\n"
        "v_lshl_add_u32 v27, v28, 5, v27\n"
        "v_bfe_u32 v28, v1, 4, 2\n"
        "v_lshl_add_u32 v27, v28, 3, v27\n"
        "v_add_u32 v26, s83, v27\n"
        "v_xor_b32 v27, 64, v27\n"
        "v_add_u32 v27, s83, v27\n"
        "s_waitcnt vmcnt(63)\n"
        "ds_read_b64_tr_b16 a[224:225], v26 offset:0\n"
        "ds_read_b64_tr_b16 a[226:227], v26 offset:1024\n"
        "ds_read_b64_tr_b16 a[228:229], v27 offset:0\n"
        "ds_read_b64_tr_b16 a[230:231], v27 offset:1024\n"
        "ds_read_b64_tr_b16 a[232:233], v26 offset:2048\n"
        "ds_read_b64_tr_b16 a[234:235], v26 offset:3072\n"
        "ds_read_b64_tr_b16 a[236:237], v27 offset:2048\n"
        "ds_read_b64_tr_b16 a[238:239], v27 offset:3072\n"
        "s_waitcnt vmcnt(63)\n"
        "ds_read_b64_tr_b16 a[240:241], v26 offset:4096\n"
        "ds_read_b64_tr_b16 a[242:243], v26 offset:5120\n"
        "ds_read_b64_tr_b16 a[244:245], v27 offset:4096\n"
        "ds_read_b64_tr_b16 a[246:247], v27 offset:5120\n"
        "s_waitcnt vmcnt(2)\n"
        "s_waitcnt lgkmcnt(8)\n"
        "v_mfma_f32_32x32x16_bf16 v[144:159], a[228:231], a[0:3], 0\n"
        "ds_read_b64_tr_b16 a[248:249], v26 offset:6144\n"
        "s_add_u32 m0, s83, 0x7000\n"
        "s_add_u32 s44, s44, 0x7000\n"
        "s_addc_u32 s45, s45, 0\n"
        "buffer_load_dwordx4 v14, s[44:47], 0 offen lds\n"
        "v_mfma_f32_32x32x16_bf16 v[160:175], a[224:227], a[4:7], 0\n"
        "ds_read_b64_tr_b16 a[250:251], v26 offset:7168\n"
        "v_bfe_u32 v22, v1, 6, 1\n"
        "v_lshlrev_b32 v22, 4, v22\n"
        "v_bfe_u32 v28, v1, 4, 2\n"
        "s_lshl_b32 s96, s85, 5\n"
        "s_lshl_b32 s97, s78, 2\n"
        "v_mfma_f32_32x32x16_bf16 v[176:191], a[228:231], a[4:7], 0\n"
        "ds_read_b64_tr_b16 a[252:253], v27 offset:6144\n"
        "s_add_u32 s96, s96, s97\n"
        "v_add3_u32 v22, v22, v28, s96\n"
        "buffer_load_dwordx4 v15, s[44:47], 0 offen offset:1024 lds\n"
        "v_mul_u32_u24 v22, 32, v22\n"
        "v_mfma_f32_32x32x16_bf16 v[128:143], a[224:227], a[0:3], 0\n"
        "ds_read_b64_tr_b16 a[254:255], v27 offset:7168\n"
        "v_add_u32 v22, 0x11000, v22\n"
        "v_add_u32 v248, v248, v249\n"
        "s_xor_b32 s96, s78, 1\n"
        "s_mul_i32 s96, s96, 40960\n"
        "s_add_u32 s96, s96, %3\n"
        "s_waitcnt lgkmcnt(8)\n"
        "v_mfma_f32_32x32x16_bf16 v[144:159], a[236:239], a[8:11], v[144:159]\n"
        "s_waitcnt vmcnt(63)\n"
        "ds_read_b64_tr_b16 a[224:225], v26 offset:8192\n"
        "buffer_load_dwordx4 v16, s[44:47], 0 offen offset:2048 lds\n"
        "v_add_u32 v7, s96, v1\n"
        "s_xor_b32 s96, s78, 2\n"
        "v_mfma_f32_32x32x16_bf16 v[160:175], a[232:235], a[12:15], v[160:175]\n"
        "ds_read_b64_tr_b16 a[226:227], v26 offset:9216\n"
        "s_mul_i32 s96, s96, 40960\n"
        "s_add_u32 s96, s96, %3\n"
        "v_add_u32 v8, s96, v1\n"
        "s_xor_b32 s96, s78, 3\n"
        "s_mul_i32 s96, s96, 40960\n"
        "v_mfma_f32_32x32x16_bf16 v[176:191], a[236:239], a[12:15], v[176:191]\n"
        "ds_read_b64_tr_b16 a[228:229], v27 offset:8192\n"
        "buffer_load_dwordx4 v17, s[44:47], 0 offen offset:3072 lds\n"
        "s_bfe_u32 s60, s20, 0x40010\n"
        "s_mul_i32 s60, s60, 57344\n"
        "s_add_u32 s96, s96, %3\n"
        "v_mfma_f32_32x32x16_bf16 v[128:143], a[232:235], a[8:11], v[128:143]\n"
        "ds_read_b64_tr_b16 a[230:231], v27 offset:9216\n"
        "s_add_u32 s44, s16, s60\n"
        "s_addc_u32 s45, s17, 0\n"
        "s_add_u32 m0, s83, 0x8000\n"
        "s_nop 0\n"
        "v_add_u32 v9, s96, v1\n"
        "s_waitcnt lgkmcnt(8)\n"
        "v_mfma_f32_32x32x16_bf16 v[144:159], a[244:247], a[16:19], v[144:159]\n"
        "ds_read_b64_tr_b16 a[232:233], v26 offset:10240\n"
        "buffer_load_dwordx4 v14, s[44:47], 0 offen lds\n"
        "v_lshlrev_b32 v28, 16, v2\n"
        "v_and_b32 v29, s90, v2\n"
        "v_lshlrev_b32 v30, 16, v18\n"
        "v_mfma_f32_32x32x16_bf16 v[160:175], a[240:243], a[20:23], v[160:175]\n"
        "ds_read_b64_tr_b16 a[234:235], v26 offset:11264\n"
        "v_and_b32 v31, s90, v18\n"
        "v_add_f32 v64, v28, v30\n"
        "v_add_f32 v65, v29, v31\n"
        "v_lshlrev_b32 v28, 16, v3\n"
        "v_and_b32 v29, s90, v3\n"
        "v_mfma_f32_32x32x16_bf16 v[176:191], a[244:247], a[20:23], v[176:191]\n"
        "ds_read_b64_tr_b16 a[236:237], v27 offset:10240\n"
        "buffer_load_dwordx4 v15, s[44:47], 0 offen offset:1024 lds\n"
        "v_lshlrev_b32 v30, 16, v19\n"
        "v_and_b32 v31, s90, v19\n"
        "v_add_f32 v66, v28, v30\n"
        "v_mfma_f32_32x32x16_bf16 v[128:143], a[240:243], a[16:19], v[128:143]\n"
        "ds_read_b64_tr_b16 a[238:239], v27 offset:11264\n"
        "v_add_f32 v67, v29, v31\n"
        "v_lshlrev_b32 v28, 16, v4\n"
        "v_and_b32 v29, s90, v4\n"
        "v_lshlrev_b32 v30, 16, v20\n"
        "v_and_b32 v31, s90, v20\n"
        "s_waitcnt lgkmcnt(8)\n"
        "v_mfma_f32_32x32x16_bf16 v[144:159], a[252:255], a[24:27], v[144:159]\n"
        "s_waitcnt vmcnt(63)\n"
        "ds_read_b64_tr_b16 a[240:241], v26 offset:12288\n"
        "buffer_load_dwordx4 v16, s[44:47], 0 offen offset:2048 lds\n"
        "v_add_f32 v68, v28, v30\n"
        "v_add_f32 v69, v29, v31\n"
        "v_mfma_f32_32x32x16_bf16 v[160:175], a[248:251], a[28:31], v[160:175]\n"
        "ds_read_b64_tr_b16 a[242:243], v26 offset:13312\n"
        "v_lshlrev_b32 v28, 16, v5\n"
        "v_and_b32 v29, s90, v5\n"
        "v_lshlrev_b32 v30, 16, v21\n"
        "v_and_b32 v31, s90, v21\n"
        "v_add_f32 v70, v28, v30\n"
        "v_mfma_f32_32x32x16_bf16 v[176:191], a[252:255], a[28:31], v[176:191]\n"
        "ds_read_b64_tr_b16 a[244:245], v27 offset:12288\n"
        "buffer_load_dwordx4 v17, s[44:47], 0 offen offset:3072 lds\n"
        "s_add_u32 m0, s83, 0x9000\n"
        "s_add_u32 s44, s44, 0x7000\n"
        "s_addc_u32 s45, s45, 0\n"
        "v_mfma_f32_32x32x16_bf16 v[128:143], a[248:251], a[24:27], v[128:143]\n"
        "ds_read_b64_tr_b16 a[246:247], v27 offset:13312\n"
        "v_add_f32 v71, v29, v31\n"
        "v_lshlrev_b32 v28, 16, v10\n"
        "v_and_b32 v29, s90, v10\n"
        "v_lshlrev_b32 v30, 16, v252\n"
        "v_and_b32 v31, s90, v252\n"
        "s_waitcnt lgkmcnt(8)\n"
        "v_mfma_f32_32x32x16_bf16 v[144:159], a[228:231], a[32:35], v[144:159]\n"
        "ds_read_b64_tr_b16 a[248:249], v26 offset:14336\n"
        "buffer_load_dwordx4 v14, s[44:47], 0 offen lds\n"
        "v_add_f32 v72, v28, v30\n"
        "v_add_f32 v73, v29, v31\n"
        "v_lshlrev_b32 v28, 16, v11\n"
        "v_mfma_f32_32x32x16_bf16 v[160:175], a[224:227], a[36:39], v[160:175]\n"
        "ds_read_b64_tr_b16 a[250:251], v26 offset:15360\n"
        "v_and_b32 v29, s90, v11\n"
        "v_lshlrev_b32 v30, 16, v253\n"
        "v_and_b32 v31, s90, v253\n"
        "v_add_f32 v74, v28, v30\n"
        "v_add_f32 v75, v29, v31\n"
        "v_mfma_f32_32x32x16_bf16 v[176:191], a[228:231], a[36:39], v[176:191]\n"
        "ds_read_b64_tr_b16 a[252:253], v27 offset:14336\n"
        "buffer_load_dwordx4 v15, s[44:47], 0 offen offset:1024 lds\n"
        "v_lshlrev_b32 v28, 16, v12\n"
        "v_and_b32 v29, s90, v12\n"
        "v_lshlrev_b32 v30, 16, v254\n"
        "v_mfma_f32_32x32x16_bf16 v[128:143], a[224:227], a[32:35], v[128:143]\n"
        "ds_read_b64_tr_b16 a[254:255], v27 offset:15360\n"
        "v_and_b32 v31, s90, v254\n"
        "v_add_f32 v76, v28, v30\n"
        "v_add_f32 v77, v29, v31\n"
        "v_lshlrev_b32 v28, 16, v13\n"
        "v_and_b32 v29, s90, v13\n"
        "s_waitcnt lgkmcnt(8)\n"
        "v_mfma_f32_32x32x16_bf16 v[144:159], a[236:239], a[40:43], v[144:159]\n"
        "s_waitcnt vmcnt(63)\n"
        "ds_read_b64_tr_b16 a[224:225], v26 offset:16384\n"
        "buffer_load_dwordx4 v16, s[44:47], 0 offen offset:2048 lds\n"
        "v_lshlrev_b32 v30, 16, v255\n"
        "v_and_b32 v31, s90, v255\n"
        "v_mfma_f32_32x32x16_bf16 v[160:175], a[232:235], a[44:47], v[160:175]\n"
        "ds_read_b64_tr_b16 a[226:227], v26 offset:17408\n"
        "v_add_f32 v78, v28, v30\n"
        "v_add_f32 v79, v29, v31\n"
        "v_mfma_f32_32x32x16_bf16 v[176:191], a[236:239], a[44:47], v[176:191]\n"
        "ds_read_b64_tr_b16 a[228:229], v27 offset:16384\n"
        "buffer_load_dwordx4 v17, s[44:47], 0 offen offset:3072 lds\n"
        "s_bfe_u32 s60, s20, 0x40014\n"
        "s_mul_i32 s60, s60, 57344\n"
        "v_mfma_f32_32x32x16_bf16 v[128:143], a[232:235], a[40:43], v[128:143]\n"
        "ds_read_b64_tr_b16 a[230:231], v27 offset:17408\n"
        "s_add_u32 s44, s16, s60\n"
        "s_addc_u32 s45, s17, 0\n"
        "s_add_u32 m0, s83, 0x0\n"
        "s_nop 0\n"
        "s_waitcnt lgkmcnt(8)\n"
        "v_mfma_f32_32x32x16_bf16 v[144:159], a[244:247], a[48:51], v[144:159]\n"
        "ds_read_b64_tr_b16 a[232:233], v26 offset:18432\n"
        "s_waitcnt vmcnt(12)\n"
        "buffer_store_dwordx2 v[24:25], v23, s[28:31], s70 offen sc1\n"
        "v_mfma_f32_32x32x16_bf16 v[160:175], a[240:243], a[52:55], v[160:175]\n"
        "ds_read_b64_tr_b16 a[234:235], v26 offset:19456\n"
        "v_mfma_f32_32x32x16_bf16 v[176:191], a[244:247], a[52:55], v[176:191]\n"
        "ds_read_b64_tr_b16 a[236:237], v27 offset:18432\n"
        "buffer_load_dwordx4 v14, s[44:47], 0 offen lds\n"
        "v_mfma_f32_32x32x16_bf16 v[128:143], a[240:243], a[48:51], v[128:143]\n"
        "ds_read_b64_tr_b16 a[238:239], v27 offset:19456\n"
        "s_waitcnt lgkmcnt(8)\n"
        "v_mfma_f32_32x32x16_bf16 v[144:159], a[252:255], a[56:59], v[144:159]\n"
        "s_waitcnt vmcnt(63)\n"
        "ds_read_b64_tr_b16 a[240:241], v26 offset:20480\n"
        "buffer_load_dwordx4 v15, s[44:47], 0 offen offset:1024 lds\n"
        "v_mfma_f32_32x32x16_bf16 v[160:175], a[248:251], a[60:63], v[160:175]\n"
        "ds_read_b64_tr_b16 a[242:243], v26 offset:21504\n"
        "v_mfma_f32_32x32x16_bf16 v[176:191], a[252:255], a[60:63], v[176:191]\n"
        "ds_read_b64_tr_b16 a[244:245], v27 offset:20480\n"
        "buffer_load_dwordx4 v16, s[44:47], 0 offen offset:2048 lds\n"
        "v_mfma_f32_32x32x16_bf16 v[128:143], a[248:251], a[56:59], v[128:143]\n"
        "ds_read_b64_tr_b16 a[246:247], v27 offset:21504\n"
        "s_waitcnt lgkmcnt(8)\n"
        "v_mfma_f32_32x32x16_bf16 v[144:159], a[228:231], a[64:67], v[144:159]\n"
        "ds_read_b64_tr_b16 a[248:249], v26 offset:22528\n"
        "buffer_load_dwordx4 v17, s[44:47], 0 offen offset:3072 lds\n"
        "s_add_u32 m0, s83, 0x1000\n"
        "s_add_u32 s44, s44, 0x7000\n"
        "s_addc_u32 s45, s45, 0\n"
        "v_mfma_f32_32x32x16_bf16 v[160:175], a[224:227], a[68:71], v[160:175]\n"
        "ds_read_b64_tr_b16 a[250:251], v26 offset:23552\n"
        "v_mfma_f32_32x32x16_bf16 v[176:191], a[228:231], a[68:71], v[176:191]\n"
        "ds_read_b64_tr_b16 a[252:253], v27 offset:22528\n"
        "buffer_load_dwordx4 v14, s[44:47], 0 offen lds\n"
        "v_mfma_f32_32x32x16_bf16 v[128:143], a[224:227], a[64:67], v[128:143]\n"
        "ds_read_b64_tr_b16 a[254:255], v27 offset:23552\n"
        "s_waitcnt lgkmcnt(8)\n"
        "v_mfma_f32_32x32x16_bf16 v[144:159], a[236:239], a[72:75], v[144:159]\n"
        "s_waitcnt vmcnt(63)\n"
        "ds_read_b64_tr_b16 a[224:225], v26 offset:24576\n"
        "buffer_load_dwordx4 v15, s[44:47], 0 offen offset:1024 lds\n"
        "v_mfma_f32_32x32x16_bf16 v[160:175], a[232:235], a[76:79], v[160:175]\n"
        "ds_read_b64_tr_b16 a[226:227], v26 offset:25600\n"
        "v_mfma_f32_32x32x16_bf16 v[176:191], a[236:239], a[76:79], v[176:191]\n"
        "ds_read_b64_tr_b16 a[228:229], v27 offset:24576\n"
        "buffer_load_dwordx4 v16, s[44:47], 0 offen offset:2048 lds\n"
        "v_mfma_f32_32x32x16_bf16 v[128:143], a[232:235], a[72:75], v[128:143]\n"
        "ds_read_b64_tr_b16 a[230:231], v27 offset:25600\n"
        "s_waitcnt lgkmcnt(8)\n"
        "v_mfma_f32_32x32x16_bf16 v[144:159], a[244:247], a[80:83], v[144:159]\n"
        "ds_read_b64_tr_b16 a[232:233], v26 offset:26624\n"
        "buffer_load_dwordx4 v17, s[44:47], 0 offen offset:3072 lds\n"
        "s_bfe_u32 s60, s20, 0x40018\n"
        "s_mul_i32 s60, s60, 57344\n"
        "v_mfma_f32_32x32x16_bf16 v[160:175], a[240:243], a[84:87], v[160:175]\n"
        "ds_read_b64_tr_b16 a[234:235], v26 offset:27648\n"
        "s_add_u32 s44, s16, s60\n"
        "s_addc_u32 s45, s17, 0\n"
        "s_add_u32 m0, s83, 0x2000\n"
        "s_nop 0\n"
        "v_mfma_f32_32x32x16_bf16 v[176:191], a[244:247], a[84:87], v[176:191]\n"
        "ds_read_b64_tr_b16 a[236:237], v27 offset:26624\n"
        "buffer_load_dwordx4 v14, s[44:47], 0 offen lds\n"
        "v_mfma_f32_32x32x16_bf16 v[128:143], a[240:243], a[80:83], v[128:143]\n"
        "ds_read_b64_tr_b16 a[238:239], v27 offset:27648\n"
        "s_waitcnt lgkmcnt(8)\n"
        "v_mfma_f32_32x32x16_bf16 v[144:159], a[252:255], a[88:91], v[144:159]\n"
        "s_waitcnt vmcnt(18)\n"
        "ds_read_b64_tr_b16 a[240:241], v26 offset:28672\n"
        "buffer_load_dwordx4 v15, s[44:47], 0 offen offset:1024 lds\n"
        "v_mfma_f32_32x32x16_bf16 v[160:175], a[248:251], a[92:95], v[160:175]\n"
        "ds_read_b64_tr_b16 a[242:243], v26 offset:29696\n"
        "v_mfma_f32_32x32x16_bf16 v[176:191], a[252:255], a[92:95], v[176:191]\n"
        "ds_read_b64_tr_b16 a[244:245], v27 offset:28672\n"
        "buffer_load_dwordx4 v16, s[44:47], 0 offen offset:2048 lds\n"
        "v_mfma_f32_32x32x16_bf16 v[128:143], a[248:251], a[88:91], v[128:143]\n"
        "ds_read_b64_tr_b16 a[246:247], v27 offset:29696\n"
        "s_waitcnt lgkmcnt(8)\n"
        "v_mfma_f32_32x32x16_bf16 v[144:159], a[228:231], a[96:99], v[144:159]\n"
        "ds_read_b64_tr_b16 a[248:249], v26 offset:30720\n"
        "buffer_load_dwordx4 v17, s[44:47], 0 offen offset:3072 lds\n"
        "s_add_u32 m0, s83, 0x3000\n"
        "s_add_u32 s44, s44, 0x7000\n"
        "s_addc_u32 s45, s45, 0\n"
        "v_mfma_f32_32x32x16_bf16 v[160:175], a[224:227], a[100:103], v[160:175]\n"
        "ds_read_b64_tr_b16 a[250:251], v26 offset:31744\n"
        "v_mfma_f32_32x32x16_bf16 v[176:191], a[228:231], a[100:103], v[176:191]\n"
        "ds_read_b64_tr_b16 a[252:253], v27 offset:30720\n"
        "buffer_load_dwordx4 v14, s[44:47], 0 offen lds\n"
        "v_mfma_f32_32x32x16_bf16 v[128:143], a[224:227], a[96:99], v[128:143]\n"
        "ds_read_b64_tr_b16 a[254:255], v27 offset:31744\n"
        "s_waitcnt lgkmcnt(8)\n"
        "v_mfma_f32_32x32x16_bf16 v[144:159], a[236:239], a[104:107], v[144:159]\n"
        "s_waitcnt vmcnt(18)\n"
        "ds_read_b64_tr_b16 a[224:225], v26 offset:32768\n"
        "buffer_load_dwordx4 v15, s[44:47], 0 offen offset:1024 lds\n"
        "v_mfma_f32_32x32x16_bf16 v[160:175], a[232:235], a[108:111], v[160:175]\n"
        "ds_read_b64_tr_b16 a[226:227], v26 offset:33792\n"
        "v_mfma_f32_32x32x16_bf16 v[176:191], a[236:239], a[108:111], v[176:191]\n"
        "ds_read_b64_tr_b16 a[228:229], v27 offset:32768\n"
        "buffer_load_dwordx4 v16, s[44:47], 0 offen offset:2048 lds\n"
        "v_mfma_f32_32x32x16_bf16 v[128:143], a[232:235], a[104:107], v[128:143]\n"
        "ds_read_b64_tr_b16 a[230:231], v27 offset:33792\n"
        "s_waitcnt lgkmcnt(8)\n"
        "v_mfma_f32_32x32x16_bf16 v[144:159], a[244:247], a[112:115], v[144:159]\n"
        "ds_read_b64_tr_b16 a[232:233], v26 offset:34816\n"
        "buffer_load_dwordx4 v17, s[44:47], 0 offen offset:3072 lds\n"
        "s_add_u32 s60, s66, 0\n"
        "s_and_b32 s60, s60, 3\n"
        "s_mul_i32 s60, s60, 25088\n"
        "v_mfma_f32_32x32x16_bf16 v[160:175], a[240:243], a[116:119], v[160:175]\n"
        "ds_read_b64_tr_b16 a[234:235], v26 offset:35840\n"
        "s_add_u32 m0, s83, 0x4000\n"
        "s_add_u32 s60, s60, s67\n"
        "v_mfma_f32_32x32x16_bf16 v[176:191], a[244:247], a[116:119], v[176:191]\n"
        "ds_read_b64_tr_b16 a[236:237], v27 offset:34816\n"
        "buffer_load_dwordx4 v14, s[48:51], s60 offen lds\n"
        "v_mfma_f32_32x32x16_bf16 v[128:143], a[240:243], a[112:115], v[128:143]\n"
        "ds_read_b64_tr_b16 a[238:239], v27 offset:35840\n"
        "s_waitcnt lgkmcnt(8)\n"
        "v_mfma_f32_32x32x16_bf16 v[144:159], a[252:255], a[120:123], v[144:159]\n"
        "s_waitcnt vmcnt(18)\n"
        "ds_read_b64_tr_b16 a[240:241], v26 offset:36864\n"
        "buffer_load_dwordx2 v[250:251], v22, s[28:31], 0 offen sc1\n"
        "v_mfma_f32_32x32x16_bf16 v[160:175], a[248:251], a[124:127], v[160:175]\n"
        "ds_read_b64_tr_b16 a[242:243], v26 offset:37888\n"
        "v_mfma_f32_32x32x16_bf16 v[176:191], a[252:255], a[124:127], v[176:191]\n"
        "ds_read_b64_tr_b16 a[244:245], v27 offset:36864\n"
        "buffer_load_dwordx4 v15, s[48:51], s60 offen offset:1024 lds\n"
        "v_mfma_f32_32x32x16_bf16 v[128:143], a[248:251], a[120:123], v[128:143]\n"
        "ds_read_b64_tr_b16 a[246:247], v27 offset:37888\n"
        "s_waitcnt lgkmcnt(8)\n"
        "v_mfma_f32_32x32x16_bf16 v[144:159], a[228:231], a[128:131], v[144:159]\n"
        "ds_read_b64_tr_b16 a[248:249], v26 offset:38912\n"
        "buffer_load_dwordx4 v16, s[48:51], s60 offen offset:2048 lds\n"
        "v_mfma_f32_32x32x16_bf16 v[160:175], a[224:227], a[132:135], v[160:175]\n"
        "ds_read_b64_tr_b16 a[250:251], v26 offset:39936\n"
        "v_mfma_f32_32x32x16_bf16 v[176:191], a[228:231], a[132:135], v[176:191]\n"
        "ds_read_b64_tr_b16 a[252:253], v27 offset:38912\n"
        "buffer_load_dwordx4 v17, s[48:51], s60 offen offset:3072 lds\n"
        "s_add_u32 s60, s66, 1\n"
        "s_and_b32 s60, s60, 3\n"
        "s_mul_i32 s60, s60, 25088\n"
        "v_mfma_f32_32x32x16_bf16 v[128:143], a[224:227], a[128:131], v[128:143]\n"
        "ds_read_b64_tr_b16 a[254:255], v27 offset:39936\n"
        "s_add_u32 m0, s83, 0x5000\n"
        "s_add_u32 s60, s60, s67\n"
        "s_waitcnt lgkmcnt(8)\n"
        "v_mfma_f32_32x32x16_bf16 v[144:159], a[236:239], a[136:139], v[144:159]\n"
        "s_waitcnt vmcnt(17)\n"
        "ds_read_b64_tr_b16 a[224:225], v26 offset:0\n"
        "buffer_load_dwordx4 v14, s[48:51], s60 offen lds\n"
        "v_mfma_f32_32x32x16_bf16 v[160:175], a[232:235], a[140:143], v[160:175]\n"
        "ds_read_b64_tr_b16 a[226:227], v26 offset:1024\n"
        "v_mfma_f32_32x32x16_bf16 v[176:191], a[236:239], a[140:143], v[176:191]\n"
        "ds_read_b64_tr_b16 a[228:229], v27 offset:0\n"
        "buffer_load_dwordx4 v15, s[48:51], s60 offen offset:1024 lds\n"
        "v_mfma_f32_32x32x16_bf16 v[128:143], a[232:235], a[136:139], v[128:143]\n"
        "ds_read_b64_tr_b16 a[230:231], v27 offset:1024\n"
        "s_waitcnt lgkmcnt(8)\n"
        "v_mfma_f32_32x32x16_bf16 v[144:159], a[244:247], a[144:147], v[144:159]\n"
        "ds_read_b64_tr_b16 a[232:233], v26 offset:2048\n"
        "buffer_load_dwordx4 v16, s[48:51], s60 offen offset:2048 lds\n"
        "v_mfma_f32_32x32x16_bf16 v[160:175], a[240:243], a[148:151], v[160:175]\n"
        "ds_read_b64_tr_b16 a[234:235], v26 offset:3072\n"
        "v_mfma_f32_32x32x16_bf16 v[176:191], a[244:247], a[148:151], v[176:191]\n"
        "ds_read_b64_tr_b16 a[236:237], v27 offset:2048\n"
        "buffer_load_dwordx4 v17, s[48:51], s60 offen offset:3072 lds\n"
        "v_mfma_f32_32x32x16_bf16 v[128:143], a[240:243], a[144:147], v[128:143]\n"
        "ds_read_b64_tr_b16 a[238:239], v27 offset:3072\n"
        "s_waitcnt lgkmcnt(8)\n"
        "v_mfma_f32_32x32x16_bf16 v[144:159], a[252:255], a[152:155], v[144:159]\n"
        "s_waitcnt vmcnt(17)\n"
        "ds_read_b64_tr_b16 a[240:241], v26 offset:4096\n"
        "s_waitcnt vmcnt(7)\n"
        "v_cmp_ne_u64 vcc, s[88:89], v[250:251]\n"
        "s_nop 1\n"
        "s_cbranch_vccnz o_spin_%=\n"
        "o_ok_%=:\n"
        "v_mfma_f32_32x32x16_bf16 v[160:175], a[248:251], a[156:159], v[160:175]\n"
        "ds_read_b64_tr_b16 a[242:243], v26 offset:5120\n"
        "s_add_u32 s61, s66, 0\n"
        "s_and_b32 s61, s61, 3\n"
        "s_lshl_b32 s61, s61, 11\n"
        "s_add_u32 s98, s61, s72\n"
        "s_add_u32 s99, s61, s73\n"
        "v_mfma_f32_32x32x16_bf16 v[176:191], a[252:255], a[156:159], v[176:191]\n"
        "ds_read_b64_tr_b16 a[244:245], v27 offset:4096\n"
        "buffer_load_dwordx4 v[32:35], v1, s[28:31], s98 offen offset:0 sc1\n"
        "v_mfma_f32_32x32x16_bf16 v[128:143], a[248:251], a[152:155], v[128:143]\n"
        "ds_read_b64_tr_b16 a[246:247], v27 offset:5120\n"
        "s_waitcnt lgkmcnt(8)\n"
        "v_mfma_f32_32x32x16_bf16 v[144:159], a[228:231], a[160:163], v[144:159]\n"
        "ds_read_b64_tr_b16 a[248:249], v26 offset:6144\n"
        "buffer_load_dwordx4 v[192:195], v1, s[28:31], s99 offen offset:0 sc1\n"
        "v_mfma_f32_32x32x16_bf16 v[160:175], a[224:227], a[164:167], v[160:175]\n"
        "ds_read_b64_tr_b16 a[250:251], v26 offset:7168\n"
        "v_mfma_f32_32x32x16_bf16 v[176:191], a[228:231], a[164:167], v[176:191]\n"
        "ds_read_b64_tr_b16 a[252:253], v27 offset:6144\n"
        "buffer_load_dwordx4 v[36:39], v1, s[28:31], s98 offen offset:1024 sc1\n"
        "v_mfma_f32_32x32x16_bf16 v[128:143], a[224:227], a[160:163], v[128:143]\n"
        "ds_read_b64_tr_b16 a[254:255], v27 offset:7168\n"
        "s_waitcnt lgkmcnt(8)\n"
        "v_mfma_f32_32x32x16_bf16 v[144:159], a[236:239], a[168:171], v[144:159]\n"
        "s_waitcnt vmcnt(63)\n"
        "ds_read_b64_tr_b16 a[224:225], v26 offset:8192\n"
        "buffer_load_dwordx4 v[196:199], v1, s[28:31], s99 offen offset:1024 sc1\n"
        "s_add_u32 s61, s66, 1\n"
        "s_and_b32 s61, s61, 3\n"
        "v_mfma_f32_32x32x16_bf16 v[160:175], a[232:235], a[172:175], v[160:175]\n"
        "ds_read_b64_tr_b16 a[226:227], v26 offset:9216\n"
        "s_lshl_b32 s61, s61, 11\n"
        "s_add_u32 s98, s61, s72\n"
        "s_add_u32 s99, s61, s73\n"
        "v_mfma_f32_32x32x16_bf16 v[176:191], a[236:239], a[172:175], v[176:191]\n"
        "ds_read_b64_tr_b16 a[228:229], v27 offset:8192\n"
        "buffer_load_dwordx4 v[40:43], v1, s[28:31], s98 offen offset:0 sc1\n"
        "v_mfma_f32_32x32x16_bf16 v[128:143], a[232:235], a[168:171], v[128:143]\n"
        "ds_read_b64_tr_b16 a[230:231], v27 offset:9216\n"
        "s_waitcnt lgkmcnt(8)\n"
        "v_mfma_f32_32x32x16_bf16 v[144:159], a[244:247], a[176:179], v[144:159]\n"
        "ds_read_b64_tr_b16 a[232:233], v26 offset:10240\n"
        "buffer_load_dwordx4 v[200:203], v1, s[28:31], s99 offen offset:0 sc1\n"
        "v_mfma_f32_32x32x16_bf16 v[160:175], a[240:243], a[180:183], v[160:175]\n"
        "ds_read_b64_tr_b16 a[234:235], v26 offset:11264\n"
        "v_mfma_f32_32x32x16_bf16 v[176:191], a[244:247], a[180:183], v[176:191]\n"
        "ds_read_b64_tr_b16 a[236:237], v27 offset:10240\n"
        "buffer_load_dwordx4 v[44:47], v1, s[28:31], s98 offen offset:1024 sc1\n"
        "v_mfma_f32_32x32x16_bf16 v[128:143], a[240:243], a[176:179], v[128:143]\n"
        "ds_read_b64_tr_b16 a[238:239], v27 offset:11264\n"
        "s_waitcnt lgkmcnt(8)\n"
        "v_mfma_f32_32x32x16_bf16 v[144:159], a[252:255], a[184:187], v[144:159]\n"
        "s_waitcnt vmcnt(63)\n"
        "ds_read_b64_tr_b16 a[240:241], v26 offset:12288\n"
        "buffer_load_dwordx4 v[204:207], v1, s[28:31], s99 offen offset:1024 sc1\n"
        "s_add_u32 s60, s66, 2\n"
        "s_and_b32 s60, s60, 3\n"
        "v_mfma_f32_32x32x16_bf16 v[160:175], a[248:251], a[188:191], v[160:175]\n"
        "ds_read_b64_tr_b16 a[242:243], v26 offset:13312\n"
        "s_mul_i32 s60, s60, 25088\n"
        "s_add_u32 m0, s83, 0x6000\n"
        "s_add_u32 s60, s60, s67\n"
        "v_mfma_f32_32x32x16_bf16 v[176:191], a[252:255], a[188:191], v[176:191]\n"
        "ds_read_b64_tr_b16 a[244:245], v27 offset:12288\n"
        "buffer_load_dwordx4 v14, s[48:51], s60 offen lds\n"
        "v_mfma_f32_32x32x16_bf16 v[128:143], a[248:251], a[184:187], v[128:143]\n"
        "ds_read_b64_tr_b16 a[246:247], v27 offset:13312\n"
        "s_waitcnt lgkmcnt(8)\n"
        "v_mfma_f32_32x32x16_bf16 v[144:159], a[228:231], a[192:195], v[144:159]\n"
        "ds_read_b64_tr_b16 a[248:249], v26 offset:14336\n"
        "buffer_load_dwordx4 v15, s[48:51], s60 offen offset:1024 lds\n"
        "v_mfma_f32_32x32x16_bf16 v[160:175], a[224:227], a[196:199], v[160:175]\n"
        "ds_read_b64_tr_b16 a[250:251], v26 offset:15360\n"
        "v_mfma_f32_32x32x16_bf16 v[176:191], a[228:231], a[196:199], v[176:191]\n"
        "ds_read_b64_tr_b16 a[252:253], v27 offset:14336\n"
        "buffer_load_dwordx4 v16, s[48:51], s60 offen offset:2048 lds\n"
        "v_mfma_f32_32x32x16_bf16 v[128:143], a[224:227], a[192:195], v[128:143]\n"
        "ds_read_b64_tr_b16 a[254:255], v27 offset:15360\n"
        "s_waitcnt lgkmcnt(8)\n"
        "v_mfma_f32_32x32x16_bf16 v[144:159], a[236:239], a[200:203], v[144:159]\n"
        "s_waitcnt vmcnt(15)\n"
        "ds_read_b64_tr_b16 a[224:225], v26 offset:16384\n"
        "buffer_load_dwordx4 v17, s[48:51], s60 offen offset:3072 lds\n"
        "s_add_u32 s61, s66, 2\n"
        "s_and_b32 s61, s61, 3\n"
        "v_mfma_f32_32x32x16_bf16 v[160:175], a[232:235], a[204:207], v[160:175]\n"
        "ds_read_b64_tr_b16 a[226:227], v26 offset:17408\n"
        "s_lshl_b32 s61, s61, 11\n"
        "s_add_u32 s98, s61, s72\n"
        "s_add_u32 s99, s61, s73\n"
        "v_mfma_f32_32x32x16_bf16 v[176:191], a[236:239], a[204:207], v[176:191]\n"
        "ds_read_b64_tr_b16 a[228:229], v27 offset:16384\n"
        "buffer_load_dwordx4 v[48:51], v1, s[28:31], s98 offen offset:0 sc1\n"
        "v_mfma_f32_32x32x16_bf16 v[128:143], a[232:235], a[200:203], v[128:143]\n"
        "ds_read_b64_tr_b16 a[230:231], v27 offset:17408\n"
        "s_waitcnt lgkmcnt(8)\n"
        "v_mfma_f32_32x32x16_bf16 v[144:159], a[244:247], a[208:211], v[144:159]\n"
        "ds_read_b64_tr_b16 a[232:233], v26 offset:18432\n"
        "buffer_load_dwordx4 v[208:211], v1, s[28:31], s99 offen offset:0 sc1\n"
        "v_mfma_f32_32x32x16_bf16 v[160:175], a[240:243], a[212:215], v[160:175]\n"
        "ds_read_b64_tr_b16 a[234:235], v26 offset:19456\n"
        "v_mfma_f32_32x32x16_bf16 v[176:191], a[244:247], a[212:215], v[176:191]\n"
        "ds_read_b64_tr_b16 a[236:237], v27 offset:18432\n"
        "buffer_load_dwordx4 v[52:55], v1, s[28:31], s98 offen offset:1024 sc1\n"
        "v_mfma_f32_32x32x16_bf16 v[128:143], a[240:243], a[208:211], v[128:143]\n"
        "ds_read_b64_tr_b16 a[238:239], v27 offset:19456\n"
        "s_waitcnt lgkmcnt(8)\n"
        "v_mfma_f32_32x32x16_bf16 v[144:159], a[252:255], a[216:219], v[144:159]\n"
        "s_waitcnt vmcnt(15)\n"
        "ds_read_b64_tr_b16 a[240:241], v26 offset:20480\n"
        "buffer_load_dwordx4 v[212:215], v1, s[28:31], s99 offen offset:1024 sc1\n"
        "s_add_u32 s60, s66, 3\n"
        "s_and_b32 s60, s60, 3\n"
        "v_mfma_f32_32x32x16_bf16 v[160:175], a[248:251], a[220:223], v[160:175]\n"
        "ds_read_b64_tr_b16 a[242:243], v26 offset:21504\n"
        "s_mul_i32 s60, s60, 25088\n"
        "s_add_u32 m0, s83, 0x7000\n"
        "s_add_u32 s60, s60, s67\n"
        "v_mfma_f32_32x32x16_bf16 v[176:191], a[252:255], a[220:223], v[176:191]\n"
        "ds_read_b64_tr_b16 a[244:245], v27 offset:20480\n"
        "buffer_load_dwordx4 v14, s[48:51], s60 offen lds\n"
        "v_mfma_f32_32x32x16_bf16 v[128:143], a[248:251], a[216:219], v[128:143]\n"
        "ds_read_b64_tr_b16 a[246:247], v27 offset:21504\n"
        "s_waitcnt vmcnt(15) lgkmcnt(8)\n"
        "v_mfma_f32_32x32x16_bf16 v[144:159], a[228:231], v[32:35], v[144:159]\n"
        "ds_read_b64_tr_b16 a[248:249], v26 offset:22528\n"
        "buffer_load_dwordx4 v15, s[48:51], s60 offen offset:1024 lds\n"
        "v_mfma_f32_32x32x16_bf16 v[160:175], a[224:227], v[192:195], v[160:175]\n"
        "ds_read_b64_tr_b16 a[250:251], v26 offset:23552\n"
        "v_mfma_f32_32x32x16_bf16 v[176:191], a[228:231], v[192:195], v[176:191]\n"
        "ds_read_b64_tr_b16 a[252:253], v27 offset:22528\n"
        "buffer_load_dwordx4 v16, s[48:51], s60 offen offset:2048 lds\n"
        "v_mfma_f32_32x32x16_bf16 v[128:143], a[224:227], v[32:35], v[128:143]\n"
        "ds_read_b64_tr_b16 a[254:255], v27 offset:23552\n"
        "s_waitcnt vmcnt(15) lgkmcnt(8)\n"
        "v_mfma_f32_32x32x16_bf16 v[144:159], a[236:239], v[36:39], v[144:159]\n"
        "s_waitcnt vmcnt(7)\n"
        "ds_read_b64_tr_b16 a[224:225], v26 offset:24576\n"
        "buffer_load_dwordx4 v17, s[48:51], s60 offen offset:3072 lds\n"
        "s_add_u32 s61, s66, 3\n"
        "s_and_b32 s61, s61, 3\n"
        "v_mfma_f32_32x32x16_bf16 v[160:175], a[232:235], v[196:199], v[160:175]\n"
        "ds_read_b64_tr_b16 a[226:227], v26 offset:25600\n"
        "s_lshl_b32 s61, s61, 11\n"
        "s_add_u32 s98, s61, s72\n"
        "s_add_u32 s99, s61, s73\n"
        "v_mfma_f32_32x32x16_bf16 v[176:191], a[236:239], v[196:199], v[176:191]\n"
        "ds_read_b64_tr_b16 a[228:229], v27 offset:24576\n"
        "buffer_load_dwordx4 v[56:59], v1, s[28:31], s98 offen offset:0 sc1\n"
        "v_mfma_f32_32x32x16_bf16 v[128:143], a[232:235], v[36:39], v[128:143]\n"
        "ds_read_b64_tr_b16 a[230:231], v27 offset:25600\n"
        "s_waitcnt vmcnt(63) lgkmcnt(8)\n"
        "v_mfma_f32_32x32x16_bf16 v[144:159], a[244:247], v[40:43], v[144:159]\n"
        "ds_read_b64_tr_b16 a[232:233], v26 offset:26624\n"
        "buffer_load_dwordx4 v[216:219], v1, s[28:31], s99 offen offset:0 sc1\n"
        "v_mfma_f32_32x32x16_bf16 v[160:175], a[240:243], v[200:203], v[160:175]\n"
        "ds_read_b64_tr_b16 a[234:235], v26 offset:27648\n"
        "v_mfma_f32_32x32x16_bf16 v[176:191], a[244:247], v[200:203], v[176:191]\n"
        "ds_read_b64_tr_b16 a[236:237], v27 offset:26624\n"
        "buffer_load_dwordx4 v[60:63], v1, s[28:31], s98 offen offset:1024 sc1\n"
        "v_mfma_f32_32x32x16_bf16 v[128:143], a[240:243], v[40:43], v[128:143]\n"
        "ds_read_b64_tr_b16 a[238:239], v27 offset:27648\n"
        "s_waitcnt vmcnt(63) lgkmcnt(8)\n"
        "v_mfma_f32_32x32x16_bf16 v[144:159], a[252:255], v[44:47], v[144:159]\n"
        "s_waitcnt vmcnt(3)\n"
        "ds_read_b64_tr_b16 a[240:241], v26 offset:28672\n"
        "buffer_load_dwordx4 v[220:223], v1, s[28:31], s99 offen offset:1024 sc1\n"
        "v_mfma_f32_32x32x16_bf16 v[160:175], a[248:251], v[204:207], v[160:175]\n"
        "ds_read_b64_tr_b16 a[242:243], v26 offset:29696\n"
        "v_mfma_f32_32x32x16_bf16 v[176:191], a[252:255], v[204:207], v[176:191]\n"
        "ds_read_b64_tr_b16 a[244:245], v27 offset:28672\n"
        "v_mfma_f32_32x32x16_bf16 v[128:143], a[248:251], v[44:47], v[128:143]\n"
        "ds_read_b64_tr_b16 a[246:247], v27 offset:29696\n"
        "s_waitcnt vmcnt(63) lgkmcnt(8)\n"
        "v_mfma_f32_32x32x16_bf16 v[144:159], a[228:231], v[48:51], v[144:159]\n"
        "ds_read_b64_tr_b16 a[248:249], v26 offset:30720\n"
        "v_mfma_f32_32x32x16_bf16 v[160:175], a[224:227], v[208:211], v[160:175]\n"
        "ds_read_b64_tr_b16 a[250:251], v26 offset:31744\n"
        "v_mfma_f32_32x32x16_bf16 v[176:191], a[228:231], v[208:211], v[176:191]\n"
        "ds_read_b64_tr_b16 a[252:253], v27 offset:30720\n"
        "v_mfma_f32_32x32x16_bf16 v[128:143], a[224:227], v[48:51], v[128:143]\n"
        "ds_read_b64_tr_b16 a[254:255], v27 offset:31744\n"
        "s_waitcnt vmcnt(63) lgkmcnt(8)\n"
        "v_mfma_f32_32x32x16_bf16 v[144:159], a[236:239], v[52:55], v[144:159]\n"
        "v_mfma_f32_32x32x16_bf16 v[160:175], a[232:235], v[212:215], v[160:175]\n"
        "v_mfma_f32_32x32x16_bf16 v[176:191], a[236:239], v[212:215], v[176:191]\n"
        "v_mfma_f32_32x32x16_bf16 v[128:143], a[232:235], v[52:55], v[128:143]\n"
        "s_waitcnt vmcnt(2) lgkmcnt(4)\n"
        "v_mfma_f32_32x32x16_bf16 v[144:159], a[244:247], v[56:59], v[144:159]\n"
        "v_mfma_f32_32x32x16_bf16 v[160:175], a[240:243], v[216:219], v[160:175]\n"
        "v_mfma_f32_32x32x16_bf16 v[176:191], a[244:247], v[216:219], v[176:191]\n"
        "v_mfma_f32_32x32x16_bf16 v[128:143], a[240:243], v[56:59], v[128:143]\n"
        "s_waitcnt vmcnt(0) lgkmcnt(0)\n"
        "v_mfma_f32_32x32x16_bf16 v[144:159], a[252:255], v[60:63], v[144:159]\n"
        "v_mfma_f32_32x32x16_bf16 v[160:175], a[248:251], v[220:223], v[160:175]\n"
        "v_mfma_f32_32x32x16_bf16 v[176:191], a[252:255], v[220:223], v[176:191]\n"
        "v_mfma_f32_32x32x16_bf16 v[128:143], a[248:251], v[60:63], v[128:143]\n"
        "s_nop 3\n"
        "s_nop 4\n"
        "ds_write_b128 v6, v[144:147] offset:0\n"
        "ds_write_b128 v6, v[148:151] offset:1024\n"
        "ds_write_b128 v6, v[152:155] offset:2048\n"
        "ds_write_b128 v6, v[156:159] offset:3072\n"
        "ds_write_b128 v6, v[160:163] offset:4096\n"
        "ds_write_b128 v6, v[164:167] offset:5120\n"
        "ds_write_b128 v6, v[168:171] offset:6144\n"
        "ds_write_b128 v6, v[172:175] offset:7168\n"
        "ds_write_b128 v6, v[176:179] offset:8192\n"
        "ds_write_b128 v6, v[180:183] offset:9216\n"
        "ds_write_b128 v6, v[184:187] offset:10240\n"
        "ds_write_b128 v6, v[188:191] offset:11264\n"
        "v_pk_add_f32 v[128:129], v[128:129], v[64:65]\n"
        "v_pk_add_f32 v[130:131], v[130:131], v[66:67]\n"
        "v_pk_add_f32 v[132:133], v[132:133], v[68:69]\n"
        "v_pk_add_f32 v[134:135], v[134:135], v[70:71]\n"
        "v_pk_add_f32 v[136:137], v[136:137], v[72:73]\n"
        "v_pk_add_f32 v[138:139], v[138:139], v[74:75]\n"
        "v_pk_add_f32 v[140:141], v[140:141], v[76:77]\n"
        "v_pk_add_f32 v[142:143], v[142:143], v[78:79]\n"
        "s_waitcnt lgkmcnt(0)\n"
        "s_barrier\n"
        "ds_read_b128 v[80:83], v7 offset:0\n"
        "ds_read_b128 v[84:87], v7 offset:1024\n"
        "ds_read_b128 v[88:91], v7 offset:2048\n"
        "ds_read_b128 v[92:95], v7 offset:3072\n"
        "ds_read_b128 v[96:99], v8 offset:4096\n"
        "ds_read_b128 v[100:103], v8 offset:5120\n"
        "ds_read_b128 v[104:107], v8 offset:6144\n"
        "ds_read_b128 v[108:111], v8 offset:7168\n"
        "ds_read_b128 v[112:115], v9 offset:8192\n"
        "ds_read_b128 v[116:119], v9 offset:9216\n"
        "ds_read_b128 v[120:123], v9 offset:10240\n"
        "ds_read_b128 v[124:127], v9 offset:11264\n"
        "s_waitcnt lgkmcnt(10)\n"
        "v_pk_add_f32 v[128:129], v[128:129], v[80:81]\n"
        "v_pk_add_f32 v[130:131], v[130:131], v[82:83]\n"
        "v_pk_add_f32 v[132:133], v[132:133], v[84:85]\n"
        "v_pk_add_f32 v[134:135], v[134:135], v[86:87]\n"
        "s_waitcnt lgkmcnt(8)\n"
        "v_pk_add_f32 v[136:137], v[136:137], v[88:89]\n"
        "v_pk_add_f32 v[138:139], v[138:139], v[90:91]\n"
        "v_pk_add_f32 v[140:141], v[140:141], v[92:93]\n"
        "v_pk_add_f32 v[142:143], v[142:143], v[94:95]\n"
        "s_waitcnt lgkmcnt(6)\n"
        "v_pk_add_f32 v[128:129], v[128:129], v[96:97]\n"
        "v_pk_add_f32 v[130:131], v[130:131], v[98:99]\n"
        "v_pk_add_f32 v[132:133], v[132:133], v[100:101]\n"
        "v_pk_add_f32 v[134:135], v[134:135], v[102:103]\n"
        "s_waitcnt lgkmcnt(4)\n"
        "v_pk_add_f32 v[136:137], v[136:137], v[104:105]\n"
        "v_pk_add_f32 v[138:139], v[138:139], v[106:107]\n"
        "v_pk_add_f32 v[140:141], v[140:141], v[108:109]\n"
        "v_pk_add_f32 v[142:143], v[142:143], v[110:111]\n"
        "s_waitcnt lgkmcnt(2)\n"
        "v_pk_add_f32 v[128:129], v[128:129], v[112:113]\n"
        "v_pk_add_f32 v[130:131], v[130:131], v[114:115]\n"
        "v_pk_add_f32 v[132:133], v[132:133], v[116:117]\n"
        "v_pk_add_f32 v[134:135], v[134:135], v[118:119]\n"
        "s_waitcnt lgkmcnt(0)\n"
        "v_pk_add_f32 v[136:137], v[136:137], v[120:121]\n"
        "v_pk_add_f32 v[138:139], v[138:139], v[122:123]\n"
        "v_pk_add_f32 v[140:141], v[140:141], v[124:125]\n"
        "v_pk_add_f32 v[142:143], v[142:143], v[126:127]\n"
        "v_cvt_pk_bf16_f32 v224, v128, v129\n"
        "v_cvt_pk_bf16_f32 v225, v130, v131\n"
        "v_cvt_pk_bf16_f32 v226, v132, v133\n"
        "v_cvt_pk_bf16_f32 v227, v134, v135\n"
        "v_cvt_pk_bf16_f32 v228, v136, v137\n"
        "v_cvt_pk_bf16_f32 v229, v138, v139\n"
        "v_cvt_pk_bf16_f32 v230, v140, v141\n"
        "v_cvt_pk_bf16_f32 v231, v142, v143\n"
        "v_permlane32_swap_b32 v224, v226\n"
        "v_permlane32_swap_b32 v225, v227\n"
        "buffer_store_dwordx4 v[224:227], v248, s[56:59], s74 offen offset:0\n"
        "v_permlane32_swap_b32 v228, v230\n"
        "v_permlane32_swap_b32 v229, v231\n"
        "buffer_store_dwordx4 v[228:231], v248, s[56:59], s74 offen offset:32\n"
        "s_endpgm\n"
        "tail_%=:\n"
        "s_waitcnt vmcnt(0)\n"
        "buffer_store_dwordx2 v[24:25], v23, s[28:31], s70 offen sc1\n"
        "s_cmp_eq_u32 %2, 224\n"
        "s_cbranch_scc0 fin_%=\n"
        "s_cmp_eq_u32 s78, 0\n"
        "s_cbranch_scc0 fin_%=\n"
        "v_lshrrev_b32 v20, 8, v1\n"
        "v_lshlrev_b32 v20, 6, v20\n"
        "v_bfe_u32 v21, v1, 4, 4\n"
        "v_add_u32 v20, v20, v21\n"
        "v_mul_u32_u24 v20, 32, v20\n"
        "v_add_u32 v32, 0x9000, v20\n"
        "v_add_u32 v33, 8192, v32\n"
        "v_add_u32 v34, 16384, v32\n"
        "v_add_u32 v35, 24576, v32\n"
        "s_branch e_spin_%=\n"
        "e_ok_%=:\n"
        "buffer_store_dwordx2 v[24:25], v23, s[28:31], 0 offen sc1\n"
        "fin_%=:\n"
        "s_endpgm\n"
        "kv_spin_%=:\n"
        "s_mov_b32 s75, 200000\n"
        "kv_loop_%=:\n"
        "s_sleep 2\n"
        "buffer_load_dwordx2 v[250:251], v22, s[28:31], 0 offen sc1\n"
        "s_waitcnt vmcnt(0)\n"
        "v_cmp_ne_u64 vcc, s[88:89], v[250:251]\n"
        "s_nop 4\n"
        "s_cbranch_vccz kv_ok_%=\n"
        "s_sub_u32 s75, s75, 1\n"
        "s_cmp_lg_u32 s75, 0\n"
        "s_cbranch_scc1 kv_loop_%=\n"
        "s_branch kv_ok_%=\n"
        "h_spin_%=:\n"
        "s_mov_b32 s75, 200000\n"
        "h_loop_%=:\n"
        "s_sleep 2\n"
        "buffer_load_dwordx2 v[250:251], v22, s[28:31], 0 offen sc1\n"
        "s_waitcnt vmcnt(0)\n"
        "v_cmp_ne_u64 vcc, s[88:89], v[250:251]\n"
        "s_nop 4\n"
        "s_cbranch_vccz h_ok_%=\n"
        "s_sub_u32 s75, s75, 1\n"
        "s_cmp_lg_u32 s75, 0\n"
        "s_cbranch_scc1 h_loop_%=\n"
        "s_branch h_ok_%=\n"
        "o_spin_%=:\n"
        "s_mov_b32 s75, 200000\n"
        "o_loop_%=:\n"
        "s_sleep 2\n"
        "buffer_load_dwordx2 v[250:251], v22, s[28:31], 0 offen sc1\n"
        "s_waitcnt vmcnt(0)\n"
        "v_cmp_ne_u64 vcc, s[88:89], v[250:251]\n"
        "s_nop 4\n"
        "s_cbranch_vccz o_ok_%=\n"
        "s_sub_u32 s75, s75, 1\n"
        "s_cmp_lg_u32 s75, 0\n"
        "s_cbranch_scc1 o_loop_%=\n"
        "s_branch o_ok_%=\n"
        "e_spin_%=:\n"
        "s_mov_b32 s75, 200000\n"
        "e_loop_%=:\n"
        "s_sleep 2\n"
        "buffer_load_dwordx2 v[40:41], v32, s[28:31], 0 offen sc1\n"
        "buffer_load_dwordx2 v[42:43], v33, s[28:31], 0 offen sc1\n"
        "buffer_load_dwordx2 v[44:45], v34, s[28:31], 0 offen sc1\n"
        "buffer_load_dwordx2 v[46:47], v35, s[28:31], 0 offen sc1\n"
        "s_waitcnt vmcnt(0)\n"
        "v_cmp_ne_u64 vcc, s[88:89], v[40:41]\n"
        "v_cmp_ne_u64 s[96:97], s[88:89], v[42:43]\n"
        "s_nop 1\n"
        "s_or_b64 vcc, vcc, s[96:97]\n"
        "v_cmp_ne_u64 s[96:97], s[88:89], v[44:45]\n"
        "s_nop 1\n"
        "s_or_b64 vcc, vcc, s[96:97]\n"
        "v_cmp_ne_u64 s[96:97], s[88:89], v[46:47]\n"
        "s_nop 1\n"
        "s_or_b64 vcc, vcc, s[96:97]\n"
        "s_nop 4\n"
        "s_cbranch_vccz e_ok_%=\n"
        "s_sub_u32 s75, s75, 1\n"
        "s_cmp_lg_u32 s75, 0\n"
        "s_cbranch_scc1 e_loop_%=\n"
        "s_branch e_ok_%=\n"
        "fb_%=:\n"
        "s_waitcnt vmcnt(0)\n"
        "v_mov_b32 v26, 0xff800000\n"
        "s_mov_b32 s98, 0x100000\n"
        "fb1_%=:\n"
        "buffer_load_dwordx4 v[32:35], v28, s[28:31], s98 offen offset:0 sc1\n"
        "buffer_load_dwordx4 v[36:39], v28, s[28:31], s98 offen offset:1024 sc1\n"
        "s_waitcnt vmcnt(0)\n"
        "v_mfma_f32_32x32x16_bf16 v[48:63], v[32:35], v[192:195], 0\n"
        "v_mfma_f32_32x32x16_bf16 v[48:63], v[36:39], v[196:199], v[48:63]\n"
        "s_nop 11\n"
        "v_max3_f32 v27, v48, v49, v50\n"
        "v_max3_f32 v30, v51, v52, v53\n"
        "v_max3_f32 v27, v27, v54, v55\n"
        "v_max3_f32 v30, v30, v56, v57\n"
        "v_max3_f32 v27, v27, v58, v59\n"
        "v_max3_f32 v30, v30, v60, v61\n"
        "v_max3_f32 v27, v27, v62, v63\n"
        "v_max3_f32 v26, v27, v30, v26\n"
        "s_add_u32 s98, s98, 65536\n"
        "s_cmp_lt_u32 s98, 0x500000\n"
        "s_cbranch_scc1 fb1_%=\n"
        "v_mov_b32 v27, v26\n"
        "s_nop 1\n"
        "v_permlane32_swap_b32 v26, v27\n"
        "v_max_f32 v26, v26, v27\n"
        "v_sub_f32 v128, 0, v26\n"
        "v_mov_b32 v80, 0\n"
        "v_sub_f32 v129, 0, v26\n"
        "v_mov_b32 v81, 0\n"
        "v_sub_f32 v130, 0, v26\n"
        "v_mov_b32 v82, 0\n"
        "v_sub_f32 v131, 0, v26\n"
        "v_mov_b32 v83, 0\n"
        "v_sub_f32 v132, 0, v26\n"
        "v_mov_b32 v84, 0\n"
        "v_sub_f32 v133, 0, v26\n"
        "v_mov_b32 v85, 0\n"
        "v_sub_f32 v134, 0, v26\n"
        "v_mov_b32 v86, 0\n"
        "v_sub_f32 v135, 0, v26\n"
        "v_mov_b32 v87, 0\n"
        "v_sub_f32 v136, 0, v26\n"
        "v_mov_b32 v88, 0\n"
        "v_sub_f32 v137, 0, v26\n"
        "v_mov_b32 v89, 0\n"
        "v_sub_f32 v138, 0, v26\n"
        "v_mov_b32 v90, 0\n"
        "v_sub_f32 v139, 0, v26\n"
        "v_mov_b32 v91, 0\n"
        "v_sub_f32 v140, 0, v26\n"
        "v_mov_b32 v92, 0\n"
        "v_sub_f32 v141, 0, v26\n"
        "v_mov_b32 v93, 0\n"
        "v_sub_f32 v142, 0, v26\n"
        "v_mov_b32 v94, 0\n"
        "v_sub_f32 v143, 0, v26\n"
        "v_mov_b32 v95, 0\n"
        "s_mov_b32 s98, 0x100000\n"
        "fb2_%=:\n"
        "buffer_load_dwordx4 v[32:35], v28, s[28:31], s98 offen offset:0 sc1\n"
        "buffer_load_dwordx4 v[36:39], v28, s[28:31], s98 offen offset:1024 sc1\n"
        "buffer_load_dwordx4 v[40:43], v29, s[28:31], s98 offen offset:0 sc1\n"
        "buffer_load_dwordx4 v[44:47], v29, s[28:31], s98 offen offset:1024 sc1\n"
        "s_waitcnt vmcnt(0)\n"
        "v_mfma_f32_32x32x16_bf16 v[48:63], v[32:35], v[192:195], v[128:143]\n"
        "v_mfma_f32_32x32x16_bf16 v[48:63], v[36:39], v[196:199], v[48:63]\n"
        "s_nop 11\n"
        "v_exp_f32 v48, v48\n"
        "v_exp_f32 v49, v49\n"
        "v_exp_f32 v50, v50\n"
        "v_exp_f32 v51, v51\n"
        "v_exp_f32 v52, v52\n"
        "v_exp_f32 v53, v53\n"
        "v_exp_f32 v54, v54\n"
        "v_exp_f32 v55, v55\n"
        "v_exp_f32 v56, v56\n"
        "v_exp_f32 v57, v57\n"
        "v_exp_f32 v58, v58\n"
        "v_exp_f32 v59, v59\n"
        "v_exp_f32 v60, v60\n"
        "v_exp_f32 v61, v61\n"
        "v_exp_f32 v62, v62\n"
        "v_exp_f32 v63, v63\n"
        "v_cvt_pk_bf16_f32 v224, v48, v49\n"
        "v_cvt_pk_bf16_f32 v225, v50, v51\n"
        "v_cvt_pk_bf16_f32 v226, v52, v53\n"
        "v_cvt_pk_bf16_f32 v227, v54, v55\n"
        "v_cvt_pk_bf16_f32 v228, v56, v57\n"
        "v_cvt_pk_bf16_f32 v229, v58, v59\n"
        "v_cvt_pk_bf16_f32 v230, v60, v61\n"
        "v_cvt_pk_bf16_f32 v231, v62, v63\n"
        "v_mfma_f32_32x32x16_bf16 v[80:95], v[40:43], v[224:227], v[80:95]\n"
        "s_nop 0\n"
        "v_mfma_f32_32x32x16_bf16 v[80:95], v[44:47], v[228:231], v[80:95]\n"
        "s_nop 7\n"
        "s_add_u32 s98, s98, 65536\n"
        "s_cmp_lt_u32 s98, 0x500000\n"
        "s_cbranch_scc1 fb2_%=\n"
        "s_nop 7\n"
        "v_mov_b32 v26, v92\n"
        "v_mov_b32 v30, v92\n"
        "s_nop 1\n"
        "v_permlane32_swap_b32 v26, v30\n"
        "v_rcp_f32 v30, v30\n"
        "s_nop 1\n"
        "s_branch norm_%=\n"
        "k_end_%=:\n"
        : "+v"(tid)
        : "s"(__builtin_amdgcn_kernarg_segment_ptr()), "s"(blockIdx.x), "s"((unsigned)(uintptr_t)lds)
        : "v1", "v2", "v3", "v4", "v5", "v6", "v7", "v8", "v9", "v10", "v11", "v12", "v13", "v14", "v15", "v16", "v17", "v18", "v19", "v20", "v21", "v22", "v23", "v24", "v25", "v26", "v27", "v28", "v29", "v30", "v31", "v32", "v33", "v34", "v35", "v36", "v37", "v38", "v39", "v40", "v41", "v42", "v43", "v44", "v45", "v46", "v47", "v48", "v49", "v50", "v51", "v52", "v53", "v54", "v55", "v56", "v57", "v58", "v59", "v60", "v61", "v62", "v63", "v64", "v65", "v66", "v67", "v68", "v69", "v70", "v71", "v72", "v73", "v74", "v75", "v76", "v77", "v78", "v79", "v80", "v81", "v82", "v83", "v84", "v85", "v86", "v87", "v88", "v89", "v90", "v91", "v92", "v93", "v94", "v95", "v96", "v97", "v98", "v99", "v100", "v101", "v102", "v103", "v104", "v105", "v106", "v107", "v108", "v109", "v110", "v111", "v112", "v113", "v114", "v115", "v116", "v117", "v118", "v119", "v120", "v121", "v122", "v123", "v124", "v125", "v126", "v127", "v128", "v129", "v130", "v131", "v132", "v133", "v134", "v135", "v136", "v137", "v138", "v139", "v140", "v141", "v142", "v143", "v144", "v145", "v146", "v147", "v148", "v149", "v150", "v151", "v152", "v153", "v154", "v155", "v156", "v157", "v158", "v159", "v160", "v161", "v162", "v163", "v164", "v165", "v166", "v167", "v168", "v169", "v170", "v171", "v172", "v173", "v174", "v175", "v176", "v177", "v178", "v179", "v180", "v181", "v182", "v183", "v184", "v185", "v186", "v187", "v188", "v189", "v190", "v191", "v192", "v193", "v194", "v195", "v196", "v197", "v198", "v199", "v200", "v201", "v202", "v203", "v204", "v205", "v206", "v207", "v208", "v209", "v210", "v211", "v212", "v213", "v214", "v215", "v216", "v217", "v218", "v219", "v220", "v221", "v222", "v223", "v224", "v225", "v226", "v227", "v228", "v229", "v230", "v231", "v232", "v233", "v234", "v235", "v236", "v237", "v238", "v239", "v240", "v241", "v242", "v243", "v244", "v245", "v246", "v247", "v248", "v249", "v250", "v251", "v252", "v253", "v254", "v255", "a0", "a1", "a2", "a3", "a4", "a5", "a6", "a7", "a8", "a9", "a10", "a11", "a12", "a13", "a14", "a15", "a16", "a17", "a18", "a19", "a20", "a21", "a22", "a23", "a24", "a25", "a26", "a27", "a28", "a29", "a30", "a31", "a32", "a33", "a34", "a35", "a36", "a37", "a38", "a39", "a40", "a41", "a42", "a43", "a44", "a45", "a46", "a47", "a48", "a49", "a50", "a51", "a52", "a53", "a54", "a55", "a56", "a57", "a58", "a59", "a60", "a61", "a62", "a63", "a64", "a65", "a66", "a67", "a68", "a69", "a70", "a71", "a72", "a73", "a74", "a75", "a76", "a77", "a78", "a79", "a80", "a81", "a82", "a83", "a84", "a85", "a86", "a87", "a88", "a89", "a90", "a91", "a92", "a93", "a94", "a95", "a96", "a97", "a98", "a99", "a100", "a101", "a102", "a103", "a104", "a105", "a106", "a107", "a108", "a109", "a110", "a111", "a112", "a113", "a114", "a115", "a116", "a117", "a118", "a119", "a120", "a121", "a122", "a123", "a124", "a125", "a126", "a127", "a128", "a129", "a130", "a131", "a132", "a133", "a134", "a135", "a136", "a137", "a138", "a139", "a140", "a141", "a142", "a143", "a144", "a145", "a146", "a147", "a148", "a149", "a150", "a151", "a152", "a153", "a154", "a155", "a156", "a157", "a158", "a159", "a160", "a161", "a162", "a163", "a164", "a165", "a166", "a167", "a168", "a169", "a170", "a171", "a172", "a173", "a174", "a175", "a176", "a177", "a178", "a179", "a180", "a181", "a182", "a183", "a184", "a185", "a186", "a187", "a188", "a189", "a190", "a191", "a192", "a193", "a194", "a195", "a196", "a197", "a198", "a199", "a200", "a201", "a202", "a203", "a204", "a205", "a206", "a207", "a208", "a209", "a210", "a211", "a212", "a213", "a214", "a215", "a216", "a217", "a218", "a219", "a220", "a221", "a222", "a223", "a224", "a225", "a226", "a227", "a228", "a229", "a230", "a231", "a232", "a233", "a234", "a235", "a236", "a237", "a238", "a239", "a240", "a241", "a242", "a243", "a244", "a245", "a246", "a247", "a248", "a249", "a250", "a251", "a252", "a253", "a254", "a255", "s4", "s5", "s6", "s7", "s8", "s9", "s10", "s11", "s12", "s13", "s14", "s15", "s16", "s17", "s18", "s19", "s20", "s21", "s22", "s23", "s24", "s25", "s26", "s27", "s28", "s29", "s30", "s31", "s32", "s33", "s34", "s35", "s36", "s37", "s38", "s39", "s40", "s41", "s42", "s43", "s44", "s45", "s46", "s47", "s48", "s49", "s50", "s51", "s52", "s53", "s54", "s55", "s56", "s57", "s58", "s59", "s60", "s61", "s62", "s63", "s64", "s65", "s66", "s67", "s68", "s69", "s70", "s71", "s72", "s73", "s74", "s75", "s76", "s77", "s78", "s79", "s80", "s81", "s82", "s83", "s84", "s85", "s86", "s87", "s88", "s89", "s90", "s91", "s92", "s93", "s94", "s95", "s96", "s97", "s98", "s99", "s100", "s101", "vcc", "memory");
}

extern "C" void kernel_launch(void* const* d_in, const int* in_sizes, int n_in, void* d_out, int out_size, void* d_ws, size_t ws_size,
                              hipStream_t stream) {
    KArgs a;
    a.x = d_in[0]; a.wqkv = d_in[1]; a.wo = d_in[2]; a.w1 = d_in[4]; a.b1 = d_in[5]; a.w2 = d_in[6]; a.b2 = d_in[7]; a.out = d_out; a.ws = d_ws;
    fused_block_kernel<<<256, 256, 0, stream>>>(a);
}
```
